```python
import torch

def _softmax(x, axis=-1):
    return torch.softmax(x, dim=axis)
def _gelu(x):
    return torch.nn.functional.gelu(x, approximate="tanh")
def _silu(x):
    return torch.nn.functional.silu(x)
def _sigmoid(x):
    return torch.sigmoid(x)
def _layernorm(x, axis=-1, eps=1e-5):
    m = x.mean(dim=axis, keepdim=True)
    v = x.var(dim=axis, keepdim=True, unbiased=False)
    return (x - m) / torch.sqrt(v + eps)
def _scatter_add(tgt, idx, src, axis=0):
    return tgt.clone().index_add_(axis, idx, src)
def _segment_sum(src, seg, n):
    out = torch.zeros((n,) + src.shape[1:], dtype=src.dtype)
    return out.index_add_(0, seg, src)
def _pad(x, pw):
    flat = tuple(v for pair in reversed(pw) for v in pair)
    return torch.nn.functional.pad(x, flat)

kernel_name = 'gated_mix__mha_block__geglu_ffn__prompt'

def setup_inputs():

    _w = (lambda a: (lambda t: t.to(torch.bfloat16) if t.is_floating_point() else t)(torch.as_tensor(a)))
    def _A_setup():
        B, S, D, H = 1, 2048, 1024, 32
        return {
            "x": _w((lambda *s: torch.randn(*s, dtype=torch.bfloat16) * 0.05)(B, S, D)),
            "wqkv": _w((lambda *s: torch.randn(*s, dtype=torch.bfloat16) * 0.8)(D, 3 * D)),
            "wo": _w((lambda *s: torch.randn(*s, dtype=torch.bfloat16) * 0.05)(D, D)),
        }
    _A_in = _A_setup()

    def _B_setup():
        B, S, D, F = 1, 2048, 1024, 3584
        return {
            "x": _w((lambda *s: torch.randn(*s, dtype=torch.bfloat16) * 0.05)(B, S, D)),
            "w_gate": _w((lambda *s: torch.randn(*s, dtype=torch.bfloat16) * 0.1)(D, F)),
            "w_up": _w((lambda *s: torch.randn(*s, dtype=torch.bfloat16) * 0.1)(D, F)),
            "w_down": _w((lambda *s: torch.randn(*s, dtype=torch.bfloat16) * 0.1)(F, D)),
        }
    _B_in = _B_setup()

    return {
        "A_x": _A_in["x"],
        "A_wqkv": _A_in["wqkv"],
        "A_wo": _A_in["wo"],
        "B_x": _B_in["x"],
        "B_w_gate": _B_in["w_gate"],
        "B_w_up": _B_in["w_up"],
        "B_w_down": _B_in["w_down"],
        "gate_w": _w((lambda *s: torch.randn(*s, dtype=torch.bfloat16) * 0.05)(1024) * 16.0),
    }

def reference(A_x, A_wqkv, A_wo, B_x, B_w_gate, B_w_up, B_w_down, gate_w):
    def _A_ref(x, wqkv, wo):
        B, S, D = x.shape
        H = 32
        Dh = D // H
        qkv = (lambda x, *d: x.permute(*d))((x @ wqkv).reshape(B, S, 3, H, Dh), 2, 0, 3, 1, 4)
        q, k, v = qkv[0], qkv[1], qkv[2]
        scores = torch.einsum("bhqd,bhkd->bhqk", q, k) * (Dh ** -0.5)
        p = _softmax(scores, axis=-1)
        o = torch.einsum("bhqk,bhkd->bhqd", p, v)
        return (lambda x, *d: x.permute(*d))(o, 0, 2, 1, 3).reshape(B, S, D) @ wo

    def _B_ref(x, w_gate, w_up, w_down):
        return (_gelu(x @ w_gate) * (x @ w_up)) @ w_down

    x = A_x
    g = _sigmoid((x * gate_w).sum(-1, keepdims=True))
    a = _A_ref(x, A_wqkv, A_wo)
    b = _B_ref(x, B_w_gate, B_w_up, B_w_down)
    return g * a + (1 - g) * b
```

```cpp
#include <hip/hip_runtime.h>
#include <hip/hip_bf16.h>
#include <stdint.h>

#define LAS __attribute__((address_space(3)))
typedef unsigned short bf16_t;
typedef short bf16x8 __attribute__((ext_vector_type(8)));
typedef short s16x4 __attribute__((ext_vector_type(4)));
typedef short v4i16_t __attribute__((ext_vector_type(4)));
typedef float f32x2 __attribute__((ext_vector_type(2)));
typedef float f32x4 __attribute__((ext_vector_type(4)));
typedef float f32x16 __attribute__((ext_vector_type(16)));
typedef unsigned u32x2 __attribute__((ext_vector_type(2)));
typedef unsigned u32x4 __attribute__((ext_vector_type(4)));
typedef __bf16 bf16x2_t __attribute__((ext_vector_type(2)));

constexpr int SEQ = 2048, DM = 1024, NQKV = 3072, FF = 3584, NH = 32, DH = 32;
constexpr int KTOT = FF + DM;
constexpr size_t WS_FLAG = 0;
constexpr size_t WS_G = 65536;
constexpr size_t WS_Q = 1u << 20;
constexpr size_t WS_K = WS_Q + (4u << 20);
constexpr size_t WS_V = WS_K + (4u << 20);
constexpr size_t WS_A = WS_V + (4u << 20);
constexpr size_t WS_P = WS_A + (20u << 20);

#define WAIT_V(n) asm volatile("s_waitcnt vmcnt(" #n ")" ::: "memory")
#define WAIT_L(n) asm volatile("s_waitcnt lgkmcnt(" #n ")" ::: "memory")
#define BAR() asm volatile("s_barrier" ::: "memory")
#define SCHED() __builtin_amdgcn_sched_barrier(0)
#define RLX_AGENT __ATOMIC_RELAXED, __HIP_MEMORY_SCOPE_AGENT

__device__ __forceinline__ unsigned cvtpk(float lo, float hi) { f32x2 v = {lo, hi}; bf16x2_t b = __builtin_convertvector(v, bf16x2_t); return __builtin_bit_cast(unsigned, b); }
__device__ __forceinline__ float bf2f(unsigned short b) { return __uint_as_float(((unsigned)b) << 16); }
__device__ __forceinline__ s16x4 trread(LAS const char* p) { return __builtin_bit_cast(s16x4, __builtin_amdgcn_ds_read_tr16_b64_v4i16((LAS v4i16_t*)p)); }
__device__ __forceinline__ bf16x8 cat8(s16x4 lo, s16x4 hi) { return (bf16x8){lo[0], lo[1], lo[2], lo[3], hi[0], hi[1], hi[2], hi[3]}; }
__device__ __forceinline__ void glds16(const char* sbase, unsigned voff, unsigned ldsaddr) {
    asm volatile("s_mov_b32 m0, %2\n\ts_nop 0\n\tglobal_load_lds_dwordx4 %0, %1" :: "v"(voff), "s"(sbase), "s"(ldsaddr) : "memory"); }
#define LDSP(a) ((LAS const char*)(uintptr_t)(unsigned)(a))
__device__ __forceinline__ float gelu_tanh(float x) {
    const float c0 = -2.0f * 0.7978845608028654f * 1.4426950408889634f, c1 = c0 * 0.044715f;
    const float z = x * __builtin_fmaf(x * x, c1, c0);
    const float e = __builtin_amdgcn_exp2f(z);
    return x * __builtin_amdgcn_rcpf(1.0f + e);
}

constexpr int K1_LDS = 2 * 32768 + 2 * 40960;
constexpr float QSCALE = 0.17677669529663687f * 1.4426950408889634f;

__global__ __launch_bounds__(512, 2) void k1_gemm(const bf16_t* __restrict__ x, const bf16_t* __restrict__ wqkv, const bf16_t* __restrict__ wg,
                                                  const bf16_t* __restrict__ wu, const bf16_t* __restrict__ gate_w, unsigned char* __restrict__ ws) {
    extern __shared__ __attribute__((aligned(16))) char smem[];
    const unsigned lds = (unsigned)(uintptr_t)(LAS char*)smem;
    const int tid = threadIdx.x, wid = __builtin_amdgcn_readfirstlane(tid >> 6), lane = tid & 63;
    const int wr = wid >> 2, wc = wid & 3, fr = lane & 15, fq = lane >> 4;
    const int bid = blockIdx.x, xcd = bid & 7, idx = bid >> 3, ctl = idx & 3, rb = idx >> 2;
    const bool type1 = ctl < 3;
    const int t1 = xcd * 3 + ctl, t2 = xcd;
    const char* gp0; const char* gp1; const char* gp2; const char* gp3; const char* gp4;
    if (type1) {
        gp0 = (const char*)(wg + 128 * t1); gp1 = (const char*)(wg + 128 * t1 + 64); gp2 = (const char*)(wu + 128 * t1); gp3 = (const char*)(wu + 128 * t1 + 64);
        gp4 = (const char*)(wqkv + 64 * t1);
    } else {
        gp0 = (const char*)(wg + 3072 + 64 * t2); gp1 = (const char*)(wu + 3072 + 64 * t2);
        gp2 = (const char*)(wqkv + 64 * (24 + 3 * t2)); gp3 = (const char*)(wqkv + 64 * (24 + 3 * t2 + 1)); gp4 = (const char*)(wqkv + 64 * (24 + 3 * t2 + 2));
    }
    const unsigned ld23 = type1 ? FF * 2 : NQKV * 2;
    const int lr = lane >> 3, ls = lane & 7;
    const unsigned voffA = (unsigned)((8 * wc + lr) * (DM * 2) + ((ls ^ ((4 * (wc & 1) + (lane >> 4)) & 7)) * 16));
    const int sB = 3 * ((lane >> 4) & 1) + 4 * (wc & 1);
    const unsigned voffG = (unsigned)((8 * wc + lr) * (FF * 2) + ((ls ^ sB) * 16));
    const unsigned voffQ = (unsigned)((8 * wc + lr) * (NQKV * 2) + ((ls ^ sB) * 16));
    const unsigned voff23 = type1 ? voffG : voffQ;
    const char* xa = (const char*)x + (size_t)rb * 256 * DM * 2;
#define K1_DMA_A(h, tt, bb) do { const char* _s = xa + (size_t)(h) * 128 * DM * 2 + (size_t)(tt) * 128; \
        _Pragma("unroll") for (int _u = 0; _u < 4; ++_u) glds16(_s + (size_t)_u * 32 * DM * 2, voffA, lds + (bb) * 32768 + ((h) * 128 + _u * 32 + wc * 8) * 128); } while (0)
#define K1_DMA_B(hb, tt, bb) do { const size_t _k = (size_t)(tt) * 64 + (hb) * 32; const unsigned _d = lds + 65536 + (bb) * 40960 + ((hb) * 32 + wc * 8) * 128; \
        glds16(gp0 + _k * (FF * 2), voffG, _d); glds16(gp1 + _k * (FF * 2), voffG, _d + 8192); \
        glds16(gp2 + _k * ld23, voff23, _d + 16384); glds16(gp3 + _k * ld23, voff23, _d + 24576); \
        glds16(gp4 + _k * (NQKV * 2), voffQ, _d + 32768); } while (0)

    if (wr == 0) { K1_DMA_B(0, 0, 0); K1_DMA_A(1, 0, 0); K1_DMA_B(0, 1, 1); }
    else         { K1_DMA_A(0, 0, 0); K1_DMA_B(1, 0, 0); K1_DMA_A(0, 1, 1); }

    {
        const int row = rb * 256 + (xcd * 4 + ctl) * 8 + wid;
        const u32x4* xr = (const u32x4*)(x + (size_t)row * DM) + lane * 2;
        const u32x4* gw = (const u32x4*)gate_w + lane * 2;
        const u32x4 a0 = xr[0], a1 = xr[1], b0 = gw[0], b1 = gw[1];
        float s = 0.f;
#pragma unroll
        for (int i = 0; i < 4; ++i) {
            s = __builtin_fmaf(__uint_as_float(a0[i] << 16), __uint_as_float(b0[i] << 16), s);
            s = __builtin_fmaf(__uint_as_float(a0[i] & 0xffff0000u), __uint_as_float(b0[i] & 0xffff0000u), s);
            s = __builtin_fmaf(__uint_as_float(a1[i] << 16), __uint_as_float(b1[i] << 16), s);
            s = __builtin_fmaf(__uint_as_float(a1[i] & 0xffff0000u), __uint_as_float(b1[i] & 0xffff0000u), s);
        }
#pragma unroll
        for (int o = 32; o >= 1; o >>= 1) s += __shfl_xor(s, o);
        if (lane == 0) ((float*)(ws + WS_G))[row] = 1.0f / (1.0f + __expf(-s));
        if (tid == 0) ((unsigned*)(ws + WS_FLAG))[bid * 64] = 0u;
    }

    const unsigned aoff0 = lds + (wr * 128 + fr) * 128 + ((fq ^ (fr >> 1)) * 16);
    const unsigned aoff1 = lds + (wr * 128 + fr) * 128 + (((fq ^ (fr >> 1)) ^ 4) * 16);
    const int q4 = (lane >> 2) & 3, p4 = lane & 3;
    const int sR = 3 * ((q4 >> 1) & 1) + 4 * (fq & 1);
    const int krow = (8 * fq + q4) * 128;
    int gA, cA, gB, cB, gS;
    if (type1) { gA = wc >> 1; cA = 4 * (wc & 1); gB = 2 + (wc >> 1); cB = cA; gS = 4; }
    else if (wc < 2) { gA = 0; cA = 4 * wc; gB = 1; cB = cA; gS = 2; }
    else { gA = wc + 1; cA = 0; gB = wc + 1; cB = 4; gS = 2; }
    const unsigned boffA0 = lds + 65536 + gA * 8192 + krow + (((cA + p4) ^ sR) * 16) + (p4 & 1) * 8;
    const unsigned boffA1 = lds + 65536 + gA * 8192 + krow + (((cA + p4) ^ sR) * 16) + ((p4 & 1) ^ 1) * 8;
    const unsigned boffB0 = lds + 65536 + gB * 8192 + krow + (((cB + p4) ^ sR) * 16) + (p4 & 1) * 8;
    const unsigned boffB1 = lds + 65536 + gB * 8192 + krow + (((cB + p4) ^ sR) * 16) + ((p4 & 1) ^ 1) * 8;
    const unsigned boffS = lds + 65536 + gS * 8192 + krow + (((2 * wc + (p4 >> 1)) ^ sR) * 16) + (p4 & 1) * 8;

    f32x4 acc[8][5];
#pragma unroll
    for (int m = 0; m < 8; ++m)
#pragma unroll
        for (int n = 0; n < 5; ++n) acc[m][n] = (f32x4){0.f, 0.f, 0.f, 0.f};
    bf16x8 af[8], bfg[5];

    WAIT_V(9); BAR();
    if (wr == 1) BAR();

#define K1_SEG(b, ks, t) do { \
        { constexpr int _bo = (b) * 40960 + (ks) * 4096; \
          bfg[0] = cat8(trread(LDSP(boffA0) + _bo), trread(LDSP(boffA0) + _bo + 512)); bfg[1] = cat8(trread(LDSP(boffA1) + _bo), trread(LDSP(boffA1) + _bo + 512)); \
          bfg[2] = cat8(trread(LDSP(boffB0) + _bo), trread(LDSP(boffB0) + _bo + 512)); bfg[3] = cat8(trread(LDSP(boffB1) + _bo), trread(LDSP(boffB1) + _bo + 512)); \
          bfg[4] = cat8(trread(LDSP(boffS) + _bo), trread(LDSP(boffS) + _bo + 512)); \
          LAS const char* _ap = LDSP((ks) ? aoff1 : aoff0) + (b) * 32768; \
          _Pragma("unroll") for (int _m = 0; _m < 8; ++_m) af[_m] = *(LAS const bf16x8*)(_ap + _m * 2048); } \
        if ((ks) == 0) { const int _tt = ((t) + 1 < 16) ? (t) + 1 : 15; if (wr == 0) K1_DMA_A(1, _tt, (b) ^ 1); else K1_DMA_B(1, _tt, (b) ^ 1); } \
        else { const int _tt = ((t) + 2 < 16) ? (t) + 2 : 15; if (wr == 0) K1_DMA_B(0, _tt, (b)); else K1_DMA_A(0, _tt, (b)); } \
        WAIT_V(9); WAIT_L(0); SCHED(); BAR(); SCHED(); \
        __builtin_amdgcn_s_setprio(1); \
        _Pragma("unroll") for (int _m = 0; _m < 8; ++_m) _Pragma("unroll") for (int _n = 0; _n < 5; ++_n) \
            acc[_m][_n] = __builtin_amdgcn_mfma_f32_16x16x32_bf16(bfg[_n], af[_m], acc[_m][_n], 0, 0, 0); \
        __builtin_amdgcn_s_setprio(0); \
        SCHED(); BAR(); SCHED(); } while (0)

    for (int t = 0; t < 16; t += 2) {
        K1_SEG(0, 0, t); K1_SEG(0, 1, t); K1_SEG(1, 0, t + 1); K1_SEG(1, 1, t + 1);
    }
    if (wr == 0) BAR();

    const bool roleP = type1 || wc < 2;
    const int row0 = rb * 256 + wr * 128 + fr;
    bf16_t* Ap = (bf16_t*)(ws + WS_A);
    bf16_t* Qb = (bf16_t*)(ws + WS_Q); bf16_t* Kb = (bf16_t*)(ws + WS_K); bf16_t* Vb = (bf16_t*)(ws + WS_V);
    const bool odd = fq & 1;
    {
        const int gam = type1 ? t1 : 24 + 3 * t2;
        const int col = 64 * gam + 16 * wc + 4 * fq;
        const int part = col >> 10, head = (col & 1023) >> 5, d = col & 31;
        const float sc = part == 0 ? QSCALE : 1.0f;
#pragma unroll
        for (int m = 0; m < 8; ++m) {
            const int row = row0 + 16 * m;
            const f32x4 v = acc[m][4] * sc;
            u32x2 w; w.x = cvtpk(v[0], v[1]); w.y = cvtpk(v[2], v[3]);
            bf16_t* dst;
            if (part == 1) dst = Kb + (size_t)head * 65536 + (row >> 6) * 2048 + (d >> 3) * 512 + (row & 63) * 8 + (d & 7);
            else dst = (part == 0 ? Qb : Vb) + ((size_t)head * SEQ + row) * 32 + d;
            *(u32x2*)dst = w;
        }
    }
    if (roleP) {
        const int hcol = (type1 ? 128 * t1 : 3072 + 64 * t2) + 32 * wc + 8 * fq;
#pragma unroll
        for (int m = 0; m < 8; ++m) {
            const int row = row0 + 16 * m;
            float h0[4], h1[4];
#pragma unroll
            for (int j = 0; j < 4; ++j) { h0[j] = gelu_tanh(acc[m][0][j]) * acc[m][2][j]; h1[j] = gelu_tanh(acc[m][1][j]) * acc[m][3][j]; }
            const unsigned a0 = cvtpk(h0[0], h0[1]), a1 = cvtpk(h0[2], h0[3]), b0 = cvtpk(h1[0], h1[1]), b1 = cvtpk(h1[2], h1[3]);
            u32x4 w; w.x = odd ? b0 : a0; w.y = odd ? b1 : a1; w.z = odd ? a0 : b0; w.w = odd ? a1 : b1;
            *(u32x4*)(Ap + (size_t)row * KTOT + hcol) = w;
        }
    } else {
#pragma unroll
        for (int pr = 0; pr < 2; ++pr) {
            const int gam = 24 + 3 * t2 + (wc - 1);
            const int col = 64 * gam + 32 * pr + 8 * fq;
            const int part = col >> 10, head = (col & 1023) >> 5, d = col & 31;
            const float sc = part == 0 ? QSCALE : 1.0f;
#pragma unroll
            for (int m = 0; m < 8; ++m) {
                const int row = row0 + 16 * m;
                const f32x4 v0 = acc[m][2 * pr] * sc, v1 = acc[m][2 * pr + 1] * sc;
                const unsigned a0 = cvtpk(v0[0], v0[1]), a1 = cvtpk(v0[2], v0[3]), b0 = cvtpk(v1[0], v1[1]), b1 = cvtpk(v1[2], v1[3]);
                u32x4 w; w.x = odd ? b0 : a0; w.y = odd ? b1 : a1; w.z = odd ? a0 : b0; w.w = odd ? a1 : b1;
                bf16_t* dst;
                if (part == 1) dst = Kb + (size_t)head * 65536 + (row >> 6) * 2048 + (d >> 3) * 512 + (row & 63) * 8;
                else dst = (part == 0 ? Qb : Vb) + ((size_t)head * SEQ + row) * 32 + d;
                *(u32x4*)dst = w;
            }
        }
    }
    WAIT_V(0);
#undef K1_SEG
#undef K1_DMA_A
#undef K1_DMA_B
}

constexpr int K2_LDS = 4 * 32768;
constexpr float THR = 8.0f;
__device__ __forceinline__ int crow(int r, int hi) { return (r & 3) + 8 * (r >> 2) + 4 * hi; }
#define MFMA32(a, b, c) __builtin_amdgcn_mfma_f32_32x32x16_bf16(a, b, c, 0, 0, 0)
#define MFMA16(a, b, c) __builtin_amdgcn_mfma_f32_16x16x32_bf16(a, b, c, 0, 0, 0)
#define MX3(a, b, c) __builtin_fmaxf(__builtin_fmaxf((a), (b)), (c))

__global__ __launch_bounds__(512, 2) void k2_attn(unsigned char* __restrict__ ws) {
    extern __shared__ __attribute__((aligned(16))) char smem[];
    const unsigned lds = (unsigned)(uintptr_t)(LAS char*)smem;
    const int tid = threadIdx.x, wid = __builtin_amdgcn_readfirstlane(tid >> 6), lane = tid & 63, r32 = lane & 31, hi = lane >> 5;
    const int bid = blockIdx.x, xcd = bid & 7, idx = bid >> 3;
    const int head = xcd * 4 + (idx & 3), qb = idx >> 2;
    const char* Kg = (const char*)(ws + WS_K) + (size_t)head * 131072;
    const char* Vg = (const char*)(ws + WS_V) + (size_t)head * 131072;
    const bf16_t* Qg = (const bf16_t*)(ws + WS_Q) + ((size_t)head * SEQ + qb * 256 + wid * 32) * 32;
    const unsigned voff = (unsigned)(wid * 2048 + lane * 16);
#define K2_DMA(j) do { const char* _k = Kg + (size_t)(j) * 16384; const char* _v = Vg + (size_t)(j) * 16384; const unsigned _d = lds + ((j) & 3) * 32768 + wid * 2048; \
        glds16(_k, voff, _d); glds16(_k + 1024, voff, _d + 1024); glds16(_v, voff, _d + 16384); glds16(_v + 1024, voff, _d + 16384 + 1024); } while (0)
    K2_DMA(0); K2_DMA(1);
    bf16x8 qf[2];
    qf[0] = *(const bf16x8*)(Qg + r32 * 32 + hi * 8);
    qf[1] = *(const bf16x8*)(Qg + r32 * 32 + 16 + hi * 8);
    const float gval = ((const float*)(ws + WS_G))[qb * 256 + wid * 32 + r32];
    bf16x8 ones;
    { const short o = (((lane >> 4) & 1) == (((lane & 15) >> 2) & 1)) ? (short)0x3F80 : (short)0;
#pragma unroll
      for (int j = 0; j < 8; ++j) ones[j] = o; }
    const int koff = hi * 1024 + r32 * 16;
    const int voffr = 16384 + ((lane >> 4) & 1) * 32 + (lane & 3) * 8 + (4 * hi + ((lane & 15) >> 2)) * 64;
    f32x16 o = {}; f32x4 lsum = {0.f, 0.f, 0.f, 0.f};
    float mhat = 0.f; f32x16 negm = {};
    bool first = true;
    for (int j = 0; j < 8; ++j) {
        if (j < 7) WAIT_V(4); else WAIT_V(0);
        BAR();
        if (j + 2 < 8) K2_DMA(j + 2);
        LAS const char* buf = LDSP(lds + (j & 3) * 32768);
#pragma unroll 1
        for (int tt = 0; tt < 4; ++tt) {
            LAS const char* kp = buf + tt * 4096 + koff;
            LAS const char* vp = buf + tt * 4096 + voffr;
            const bf16x8 k00 = *(LAS const bf16x8*)(kp), k01 = *(LAS const bf16x8*)(kp + 2048), k10 = *(LAS const bf16x8*)(kp + 512), k11 = *(LAS const bf16x8*)(kp + 2048 + 512);
            f32x16 p0 = MFMA32(k00, qf[0], negm); p0 = MFMA32(k01, qf[1], p0);
            f32x16 p1 = MFMA32(k10, qf[0], negm); p1 = MFMA32(k11, qf[1], p1);
            float a = MX3(p0[0], p0[1], p1[0]), b = MX3(p0[2], p0[3], p1[1]); a = MX3(a, p1[2], p1[3]);
#pragma unroll
            for (int r = 4; r < 16; r += 4) { a = MX3(a, p0[r], p0[r + 1]); b = MX3(b, p0[r + 2], p0[r + 3]); a = MX3(a, p1[r], p1[r + 1]); b = MX3(b, p1[r + 2], p1[r + 3]); }
            float rm = __builtin_fmaxf(a, b);
            { auto rr = __builtin_amdgcn_permlane32_swap(__float_as_uint(rm), __float_as_uint(rm), false, false); rm = __builtin_fmaxf(__uint_as_float(rr[0]), __uint_as_float(rr[1])); }
            if (first || __any(rm > THR)) {
                const float dl = first ? rm : __builtin_fmaxf(rm, 0.f);
                mhat += dl;
#pragma unroll
                for (int r = 0; r < 16; ++r) { p0[r] -= dl; p1[r] -= dl; negm[r] = -mhat; }
                if (!first) {
                    const float f = __builtin_amdgcn_exp2f(-dl);
                    o = o * f; lsum = lsum * f;
                }
                first = false;
            }
#pragma unroll
            for (int r = 0; r < 16; ++r) { p0[r] = __builtin_amdgcn_exp2f(p0[r]); p1[r] = __builtin_amdgcn_exp2f(p1[r]); }
            u32x4 w0 = {cvtpk(p0[0], p0[1]), cvtpk(p0[2], p0[3]), cvtpk(p0[4], p0[5]), cvtpk(p0[6], p0[7])};
            u32x4 w1 = {cvtpk(p0[8], p0[9]), cvtpk(p0[10], p0[11]), cvtpk(p0[12], p0[13]), cvtpk(p0[14], p0[15])};
            u32x4 w2 = {cvtpk(p1[0], p1[1]), cvtpk(p1[2], p1[3]), cvtpk(p1[4], p1[5]), cvtpk(p1[6], p1[7])};
            u32x4 w3 = {cvtpk(p1[8], p1[9]), cvtpk(p1[10], p1[11]), cvtpk(p1[12], p1[13]), cvtpk(p1[14], p1[15])};
            const bf16x8 v0 = cat8(trread(vp), trread(vp + 512)), v1 = cat8(trread(vp + 1024), trread(vp + 1536));
            const bf16x8 v2 = cat8(trread(vp + 2048), trread(vp + 2560)), v3 = cat8(trread(vp + 3072), trread(vp + 3584));
            const bf16x8 f0 = __builtin_bit_cast(bf16x8, w0), f1 = __builtin_bit_cast(bf16x8, w1), f2 = __builtin_bit_cast(bf16x8, w2), f3 = __builtin_bit_cast(bf16x8, w3);
            o = MFMA32(v0, f0, o); lsum = MFMA16(ones, f0, lsum);
            o = MFMA32(v1, f1, o); lsum = MFMA16(ones, f1, lsum);
            o = MFMA32(v2, f2, o); lsum = MFMA16(ones, f2, lsum);
            o = MFMA32(v3, f3, o); lsum = MFMA16(ones, f3, lsum);
        }
    }
    WAIT_V(0);
    const float sc = gval * __builtin_amdgcn_rcpf(lsum[0]);
    bf16_t* Op = (bf16_t*)(ws + WS_A) + (size_t)(qb * 256 + wid * 32 + r32) * KTOT + FF + head * 32 + 4 * hi;
#pragma unroll
    for (int g = 0; g < 4; ++g) {
        u32x2 w; w.x = cvtpk(o[4 * g] * sc, o[4 * g + 1] * sc); w.y = cvtpk(o[4 * g + 2] * sc, o[4 * g + 3] * sc);
        *(u32x2*)(Op + 8 * g) = w;
    }
#undef K2_DMA
}

constexpr int K3_NS = 4;
constexpr int K3_LDS = K3_NS * 32768;
__global__ __launch_bounds__(512, 2) void k3_gemm(const bf16_t* __restrict__ wo, const bf16_t* __restrict__ wd, unsigned char* __restrict__ ws, bf16_t* __restrict__ out) {
    extern __shared__ __attribute__((aligned(16))) char smem[];
    const unsigned lds = (unsigned)(uintptr_t)(LAS char*)smem;
    const int tid = threadIdx.x, wid = __builtin_amdgcn_readfirstlane(tid >> 6), lane = tid & 63;
    const int G = wid >> 2, j4 = wid & 3, wm = j4 >> 1, wn = j4 & 1, fr = lane & 15, fq = lane >> 4;
    const int bid = blockIdx.x, xcd = bid & 7, idx = bid >> 3;
    const int kh = xcd >> 2, mb = (xcd & 3) * 4 + (idx >> 3), nb = idx & 7;
    const int kbase = kh ? 0 : 2304;
    const char* Ag = (const char*)(ws + WS_A) + ((size_t)mb * 128 * KTOT + kbase) * 2;
    const int lr = lane >> 3, ls = lane & 7;
    const unsigned voffA = (unsigned)((8 * j4 + lr) * (KTOT * 2) + ((ls ^ ((4 * (j4 & 1) + (lane >> 4)) & 7)) * 16));
    const int sB = 3 * ((lane >> 4) & 1) + 4 * (j4 & 1);
    const unsigned voffB = (unsigned)((8 * j4 + lr) * (DM * 2) + ((ls ^ sB) * 16));
#define K3_DMA(tt) do { const int _t = (tt) < 36 ? (tt) : 35; const int _k = kbase + _t * 64; const unsigned _d = lds + ((tt) % K3_NS) * 32768; \
        const char* _a = Ag + (size_t)_t * 128; \
        _Pragma("unroll") for (int _u = 0; _u < 4; ++_u) glds16(_a + (size_t)_u * 32 * KTOT * 2, voffA, _d + (_u * 32 + j4 * 8) * 128); \
        const char* _w = (_k < FF ? (const char*)wd + (size_t)_k * DM * 2 : (const char*)wo + (size_t)(_k - FF) * DM * 2) + nb * 256; \
        _Pragma("unroll") for (int _u = 0; _u < 2; ++_u) { glds16(_w + (size_t)_u * 32 * DM * 2, voffB, _d + 16384 + (_u * 32 + j4 * 8) * 128); \
                                                          glds16(_w + (size_t)_u * 32 * DM * 2 + 128, voffB, _d + 16384 + 8192 + (_u * 32 + j4 * 8) * 128); } } while (0)
    if (K3_NS == 4) { if (G == 1) { K3_DMA(0); K3_DMA(2); } else { K3_DMA(1); } }
    const float* gv = (const float*)(ws + WS_G) + mb * 128 + wm * 64 + fr;
    float omg[4];
#pragma unroll
    for (int m = 0; m < 4; ++m) omg[m] = 1.0f - gv[16 * m];

    const int aoff0 = (wm * 64 + fr) * 128 + ((fq ^ (fr >> 1)) * 16);
    const int aoff1 = aoff0 ^ 64;
    const int q4 = (lane >> 2) & 3, p4 = lane & 3;
    const int sR = 3 * ((q4 >> 1) & 1) + 4 * (fq & 1);
    const int krow = (8 * fq + q4) * 128;
    const int boff00 = 16384 + wn * 8192 + krow + ((p4 ^ sR) * 16) + (p4 & 1) * 8, boff01 = boff00 ^ 8;
    const int boff10 = 16384 + wn * 8192 + krow + (((4 + p4) ^ sR) * 16) + (p4 & 1) * 8, boff11 = boff10 ^ 8;
    f32x4 acc[4][4];
#pragma unroll
    for (int m = 0; m < 4; ++m)
#pragma unroll
        for (int n = 0; n < 4; ++n) acc[m][n] = (f32x4){0.f, 0.f, 0.f, 0.f};
    bf16x8 af[4][2], bfg[4][2];
    WAIT_V(0); BAR();
    if (G == 1) BAR();
    for (int t = G; t < 36; t += 2) {
        LAS const char* sb = LDSP(lds + (t % K3_NS) * 32768);
#pragma unroll
        for (int ks = 0; ks < 2; ++ks) {
            LAS const char* bp = sb + ks * 4096;
            bfg[0][ks] = cat8(trread(bp + boff00), trread(bp + boff00 + 512)); bfg[1][ks] = cat8(trread(bp + boff01), trread(bp + boff01 + 512));
            bfg[2][ks] = cat8(trread(bp + boff10), trread(bp + boff10 + 512)); bfg[3][ks] = cat8(trread(bp + boff11), trread(bp + boff11 + 512));
#pragma unroll
            for (int m = 0; m < 4; ++m) af[m][ks] = *(LAS const bf16x8*)(sb + (ks ? aoff1 : aoff0) + m * 2048);
        }
        K3_DMA(t + K3_NS - 1);
        WAIT_V(8); WAIT_L(0); SCHED(); BAR(); SCHED();
        if (kh == 0 && (t == 20 || t == 21)) {
#pragma unroll
            for (int m = 0; m < 4; ++m)
#pragma unroll
                for (int n = 0; n < 4; ++n) acc[m][n] = acc[m][n] * omg[m];
        }
        __builtin_amdgcn_s_setprio(1);
#pragma unroll
        for (int ks = 0; ks < 2; ++ks)
#pragma unroll
            for (int m = 0; m < 4; ++m)
#pragma unroll
                for (int n = 0; n < 4; ++n) acc[m][n] = MFMA16(bfg[n][ks], af[m][ks], acc[m][n]);
        __builtin_amdgcn_s_setprio(0);
        SCHED(); BAR(); SCHED();
    }
    if (G == 0) BAR();
    if (kh == 1) {
#pragma unroll
        for (int m = 0; m < 4; ++m)
#pragma unroll
            for (int n = 0; n < 4; ++n) acc[m][n] = acc[m][n] * omg[m];
    }
    WAIT_V(0); BAR();
    f32x4 keep[2][4];
    {
        LAS f32x4* xs = (LAS f32x4*)(uintptr_t)(lds + wid * 8192);
        LAS const f32x4* xr = (LAS const f32x4*)(uintptr_t)(lds + (wid ^ 4) * 8192);
        if (G == 0) {
#pragma unroll
            for (int mm = 0; mm < 2; ++mm)
#pragma unroll
                for (int n = 0; n < 4; ++n) { xs[(mm * 4 + n) * 64 + lane] = acc[2 + mm][n]; keep[mm][n] = acc[mm][n]; }
        } else {
#pragma unroll
            for (int mm = 0; mm < 2; ++mm)
#pragma unroll
                for (int n = 0; n < 4; ++n) { xs[(mm * 4 + n) * 64 + lane] = acc[mm][n]; keep[mm][n] = acc[2 + mm][n]; }
        }
        WAIT_L(0); BAR();
#pragma unroll
        for (int mm = 0; mm < 2; ++mm)
#pragma unroll
            for (int n = 0; n < 4; ++n) keep[mm][n] += xr[(mm * 4 + n) * 64 + lane];
    }
    f32x4 snd[4], fin[4];
    if (kh) {
#pragma unroll
        for (int n = 0; n < 4; ++n) { snd[n] = keep[0][n]; fin[n] = keep[1][n]; }
    } else {
#pragma unroll
        for (int n = 0; n < 4; ++n) { snd[n] = keep[1][n]; fin[n] = keep[0][n]; }
    }
    const int pbid = bid ^ 4;
    {
        const __amdgpu_buffer_rsrc_t rs = __builtin_amdgcn_make_buffer_rsrc((void*)((float*)(ws + WS_P)), (short)0, (int)(256 * 32768), 0x00020000);
        const unsigned so = (unsigned)(bid * 32768 + wid * 4096 + lane * 16);
#pragma unroll
        for (int n = 0; n < 4; ++n) __builtin_amdgcn_raw_buffer_store_b128(__builtin_bit_cast(u32x4, snd[n]), rs, so + n * 1024, 0, 16);
        WAIT_V(0); BAR();
        unsigned* flags = (unsigned*)(ws + WS_FLAG);
        if (tid == 0) {
            __hip_atomic_store(flags + bid * 64, 1u, RLX_AGENT);
            unsigned spins = 0;
            while (__hip_atomic_load(flags + pbid * 64, RLX_AGENT) == 0u) { __builtin_amdgcn_s_sleep(1); if (++spins > (1u << 22)) break; }
        }
        BAR();
        const unsigned po = (unsigned)(pbid * 32768 + wid * 4096 + lane * 16);
#pragma unroll
        for (int n = 0; n < 4; ++n) fin[n] += __builtin_bit_cast(f32x4, __builtin_amdgcn_raw_buffer_load_b128(rs, po + n * 1024, 0, 16));
        const int row = mb * 128 + wm * 64 + (2 * G + kh) * 16 + fr;
        const bool odd = fq & 1;
#pragma unroll
        for (int pr = 0; pr < 2; ++pr) {
            const f32x4 v0 = fin[2 * pr], v1 = fin[2 * pr + 1];
            const unsigned a0 = cvtpk(v0[0], v0[1]), a1 = cvtpk(v0[2], v0[3]), b0 = cvtpk(v1[0], v1[1]), b1 = cvtpk(v1[2], v1[3]);
            u32x4 w; w.x = odd ? b0 : a0; w.y = odd ? b1 : a1; w.z = odd ? a0 : b0; w.w = odd ? a1 : b1;
            *(u32x4*)(out + (size_t)row * DM + nb * 128 + wn * 64 + pr * 32 + 8 * fq) = w;
        }
    }
#undef K3_DMA
}

extern "C" void kernel_launch(void* const* d_in, const int* in_sizes, int n_in, void* d_out, int out_size, void* d_ws, size_t ws_size, hipStream_t stream) {
    const bf16_t* x = (const bf16_t*)d_in[0];
    const bf16_t* wqkv = (const bf16_t*)d_in[1];
    const bf16_t* wo = (const bf16_t*)d_in[2];
    const bf16_t* wg = (const bf16_t*)d_in[4];
    const bf16_t* wu = (const bf16_t*)d_in[5];
    const bf16_t* wd = (const bf16_t*)d_in[6];
    const bf16_t* gw = (const bf16_t*)d_in[7];
    unsigned char* ws = (unsigned char*)d_ws;
    (void)hipFuncSetAttribute((const void*)k1_gemm, hipFuncAttributeMaxDynamicSharedMemorySize, K1_LDS);
    (void)hipFuncSetAttribute((const void*)k2_attn, hipFuncAttributeMaxDynamicSharedMemorySize, K2_LDS);
    (void)hipFuncSetAttribute((const void*)k3_gemm, hipFuncAttributeMaxDynamicSharedMemorySize, K3_LDS);
    k1_gemm<<<256, 512, K1_LDS, stream>>>(x, wqkv, wg, wu, gw, ws);
    k2_attn<<<256, 512, K2_LDS, stream>>>(ws);
    k3_gemm<<<256, 512, K3_LDS, stream>>>(wo, wd, ws, (bf16_t*)d_out);
}
```

```cpp
#include <hip/hip_runtime.h>
#include <hip/hip_bf16.h>
#include <stdint.h>

#define LAS __attribute__((address_space(3)))
typedef unsigned short bf16_t;
typedef short bf16x8 __attribute__((ext_vector_type(8)));
typedef short s16x4 __attribute__((ext_vector_type(4)));
typedef short v4i16_t __attribute__((ext_vector_type(4)));
typedef float f32x2 __attribute__((ext_vector_type(2)));
typedef float f32x4 __attribute__((ext_vector_type(4)));
typedef float f32x16 __attribute__((ext_vector_type(16)));
typedef unsigned u32x2 __attribute__((ext_vector_type(2)));
typedef unsigned u32x4 __attribute__((ext_vector_type(4)));
typedef __bf16 bf16x2_t __attribute__((ext_vector_type(2)));

constexpr int SEQ = 2048, DM = 1024, NQKV = 3072, FF = 3584, NH = 32, DH = 32;
constexpr int KTOT = FF + DM;
constexpr size_t WS_FLAG = 0;
constexpr size_t WS_G = 131072;
constexpr size_t WS_Q = 1u << 20;
constexpr size_t WS_K = WS_Q + (4u << 20);
constexpr size_t WS_V = WS_K + (4u << 20);
constexpr size_t WS_A = WS_V + (4u << 20);
constexpr size_t WS_P = WS_A + (20u << 20);

#define WAIT_V(n) asm volatile("s_waitcnt vmcnt(" #n ")" ::: "memory")
#define WAIT_L(n) asm volatile("s_waitcnt lgkmcnt(" #n ")" ::: "memory")
#define BAR() asm volatile("s_barrier" ::: "memory")
#define SCHED() __builtin_amdgcn_sched_barrier(0)
#define RLX_AGENT __ATOMIC_RELAXED, __HIP_MEMORY_SCOPE_AGENT
#define FINE 0
#define STAMP(kern, i) do {} while (0)
#define FSTAMP(cond, i) do {} while (0)

__device__ __forceinline__ unsigned cvtpk(float lo, float hi) { f32x2 v = {lo, hi}; bf16x2_t b = __builtin_convertvector(v, bf16x2_t); return __builtin_bit_cast(unsigned, b); }
__device__ __forceinline__ float bf2f(unsigned short b) { return __uint_as_float(((unsigned)b) << 16); }
__device__ __forceinline__ s16x4 trread(LAS const char* p) { return __builtin_bit_cast(s16x4, __builtin_amdgcn_ds_read_tr16_b64_v4i16((LAS v4i16_t*)p)); }
__device__ __forceinline__ bf16x8 cat8(s16x4 lo, s16x4 hi) { return (bf16x8){lo[0], lo[1], lo[2], lo[3], hi[0], hi[1], hi[2], hi[3]}; }
__device__ __forceinline__ void glds16(const char* sbase, unsigned voff, unsigned ldsaddr) {
    asm volatile("s_mov_b32 m0, %2\n\ts_nop 0\n\tglobal_load_lds_dwordx4 %0, %1" :: "v"(voff), "s"(sbase), "s"(ldsaddr) : "memory"); }
__device__ __forceinline__ void glds16_sc1(const char* sbase, unsigned voff, unsigned ldsaddr) {
    asm volatile("s_mov_b32 m0, %2\n\ts_nop 0\n\tglobal_load_lds_dwordx4 %0, %1 sc1" :: "v"(voff), "s"(sbase), "s"(ldsaddr) : "memory"); }
#define LDSP(a) ((LAS const char*)(uintptr_t)(unsigned)(a))
__device__ __forceinline__ float gelu_tanh(float x) {
    const float c0 = -2.0f * 0.7978845608028654f * 1.4426950408889634f, c1 = c0 * 0.044715f;
    const float z = x * __builtin_fmaf(x * x, c1, c0);
    const float e = __builtin_amdgcn_exp2f(z);
    return x * __builtin_amdgcn_rcpf(1.0f + e);
}

constexpr int K1_LDS = 2 * 32768 + 2 * 40960;
constexpr float QSCALE = 0.17677669529663687f * 1.4426950408889634f;

__global__ __launch_bounds__(512, 2) void k1_gemm(const bf16_t* __restrict__ x, const bf16_t* __restrict__ wqkv, const bf16_t* __restrict__ wg,
                                                  const bf16_t* __restrict__ wu, const bf16_t* __restrict__ gate_w, unsigned char* __restrict__ ws) {
    extern __shared__ __attribute__((aligned(16))) char smem[];
    const unsigned lds = (unsigned)(uintptr_t)(LAS char*)smem;
    const int tid = threadIdx.x, wid = __builtin_amdgcn_readfirstlane(tid >> 6), lane = tid & 63;
    const int wr = wid >> 2, wc = wid & 3, fr = lane & 15, fq = lane >> 4;
    const int bid = blockIdx.x, xcd = bid & 7, idx = bid >> 3, ctl = idx & 3, rb = idx >> 2;
    const bool type1 = ctl < 3;
    const int t1 = xcd * 3 + ctl, t2 = xcd;
    const char* gp0; const char* gp1; const char* gp2; const char* gp3; const char* gp4;
    if (type1) {
        gp0 = (const char*)(wg + 128 * t1); gp1 = (const char*)(wg + 128 * t1 + 64); gp2 = (const char*)(wu + 128 * t1); gp3 = (const char*)(wu + 128 * t1 + 64);
        gp4 = (const char*)(wqkv + 64 * t1);
    } else {
        gp0 = (const char*)(wg + 3072 + 64 * t2); gp1 = (const char*)(wu + 3072 + 64 * t2);
        gp2 = (const char*)(wqkv + 64 * (24 + 3 * t2)); gp3 = (const char*)(wqkv + 64 * (24 + 3 * t2 + 1)); gp4 = (const char*)(wqkv + 64 * (24 + 3 * t2 + 2));
    }
    const unsigned ld23 = type1 ? FF * 2 : NQKV * 2;
    const int lr = lane >> 3, ls = lane & 7;
    const unsigned voffA = (unsigned)((8 * wc + lr) * (DM * 2) + ((ls ^ ((4 * (wc & 1) + (lane >> 4)) & 7)) * 16));
    const int sB = 3 * ((lane >> 4) & 1) + 4 * (wc & 1);
    const unsigned voffG = (unsigned)((8 * wc + lr) * (FF * 2) + ((ls ^ sB) * 16));
    const unsigned voffQ = (unsigned)((8 * wc + lr) * (NQKV * 2) + ((ls ^ sB) * 16));
    const unsigned voff23 = type1 ? voffG : voffQ;
    const char* xa = (const char*)x + (size_t)rb * 256 * DM * 2;
#define K1_DMA_A(h, tt, bb) do { const char* _s = xa + (size_t)(h) * 128 * DM * 2 + (size_t)(tt) * 128; \
        _Pragma("unroll") for (int _u = 0; _u < 4; ++_u) glds16(_s + (size_t)_u * 32 * DM * 2, voffA, lds + (bb) * 32768 + ((h) * 128 + _u * 32 + wc * 8) * 128); } while (0)
#define K1_DMA_B(hb, tt, bb) do { const size_t _k = (size_t)(tt) * 64 + (hb) * 32; const unsigned _d = lds + 65536 + (bb) * 40960 + ((hb) * 32 + wc * 8) * 128; \
        glds16(gp0 + _k * (FF * 2), voffG, _d); glds16(gp1 + _k * (FF * 2), voffG, _d + 8192); \
        glds16(gp2 + _k * ld23, voff23, _d + 16384); glds16(gp3 + _k * ld23, voff23, _d + 24576); \
        glds16(gp4 + _k * (NQKV * 2), voffQ, _d + 32768); } while (0)

    STAMP(0, 0);
    if (wr == 0) { K1_DMA_B(0, 0, 0); K1_DMA_A(1, 0, 0); K1_DMA_B(0, 1, 1); }
    else         { K1_DMA_A(0, 0, 0); K1_DMA_B(1, 0, 0); K1_DMA_A(0, 1, 1); }

    {
        const int row = rb * 256 + (xcd * 4 + ctl) * 8 + wid;
        const u32x4* xr = (const u32x4*)(x + (size_t)row * DM) + lane * 2;
        const u32x4* gw = (const u32x4*)gate_w + lane * 2;
        const u32x4 a0 = xr[0], a1 = xr[1], b0 = gw[0], b1 = gw[1];
        float s = 0.f;
#pragma unroll
        for (int i = 0; i < 4; ++i) {
            s = __builtin_fmaf(__uint_as_float(a0[i] << 16), __uint_as_float(b0[i] << 16), s);
            s = __builtin_fmaf(__uint_as_float(a0[i] & 0xffff0000u), __uint_as_float(b0[i] & 0xffff0000u), s);
            s = __builtin_fmaf(__uint_as_float(a1[i] << 16), __uint_as_float(b1[i] << 16), s);
            s = __builtin_fmaf(__uint_as_float(a1[i] & 0xffff0000u), __uint_as_float(b1[i] & 0xffff0000u), s);
        }
#pragma unroll
        for (int o = 32; o >= 1; o >>= 1) s += __shfl_xor(s, o);
        if (lane == 0) ((float*)(ws + WS_G))[row] = 1.0f / (1.0f + __expf(-s));
        if (tid == 0) ((unsigned*)(ws + WS_FLAG))[bid * 64] = 0u;
        if (tid == 64 && bid < 32) ((unsigned*)(ws + WS_FLAG))[(256 + bid) * 64] = 0u;
    }

    const unsigned aoff0 = lds + (wr * 128 + fr) * 128 + ((fq ^ (fr >> 1)) * 16);
    const unsigned aoff1 = lds + (wr * 128 + fr) * 128 + (((fq ^ (fr >> 1)) ^ 4) * 16);
    const int q4 = (lane >> 2) & 3, p4 = lane & 3;
    const int sR = 3 * ((q4 >> 1) & 1) + 4 * (fq & 1);
    const int krow = (8 * fq + q4) * 128;
    int gA, cA, gB, cB, gS;
    if (type1) { gA = wc >> 1; cA = 4 * (wc & 1); gB = 2 + (wc >> 1); cB = cA; gS = 4; }
    else if (wc < 2) { gA = 0; cA = 4 * wc; gB = 1; cB = cA; gS = 2; }
    else { gA = wc + 1; cA = 0; gB = wc + 1; cB = 4; gS = 2; }
    const unsigned boffA0 = lds + 65536 + gA * 8192 + krow + (((cA + p4) ^ sR) * 16) + (p4 & 1) * 8;
    const unsigned boffA1 = lds + 65536 + gA * 8192 + krow + (((cA + p4) ^ sR) * 16) + ((p4 & 1) ^ 1) * 8;
    const unsigned boffB0 = lds + 65536 + gB * 8192 + krow + (((cB + p4) ^ sR) * 16) + (p4 & 1) * 8;
    const unsigned boffB1 = lds + 65536 + gB * 8192 + krow + (((cB + p4) ^ sR) * 16) + ((p4 & 1) ^ 1) * 8;
    const unsigned boffS = lds + 65536 + gS * 8192 + krow + (((2 * wc + (p4 >> 1)) ^ sR) * 16) + (p4 & 1) * 8;

    f32x4 acc[8][5];
#pragma unroll
    for (int m = 0; m < 8; ++m)
#pragma unroll
        for (int n = 0; n < 5; ++n) acc[m][n] = (f32x4){0.f, 0.f, 0.f, 0.f};
    bf16x8 af[8], bfg[5];

    WAIT_V(9); BAR();
    STAMP(0, 1);
    if (wr == 1) BAR();

#define K1_SEG(b, ks, t) do { \
        { constexpr int _bo = (b) * 40960 + (ks) * 4096; \
          bfg[0] = cat8(trread(LDSP(boffA0) + _bo), trread(LDSP(boffA0) + _bo + 512)); bfg[1] = cat8(trread(LDSP(boffA1) + _bo), trread(LDSP(boffA1) + _bo + 512)); \
          bfg[2] = cat8(trread(LDSP(boffB0) + _bo), trread(LDSP(boffB0) + _bo + 512)); bfg[3] = cat8(trread(LDSP(boffB1) + _bo), trread(LDSP(boffB1) + _bo + 512)); \
          bfg[4] = cat8(trread(LDSP(boffS) + _bo), trread(LDSP(boffS) + _bo + 512)); \
          LAS const char* _ap = LDSP((ks) ? aoff1 : aoff0) + (b) * 32768; \
          _Pragma("unroll") for (int _m = 0; _m < 8; ++_m) af[_m] = *(LAS const bf16x8*)(_ap + _m * 2048); } \
        if ((ks) == 0) { const int _tt = ((t) + 1 < 16) ? (t) + 1 : 15; if (wr == 0) K1_DMA_A(1, _tt, (b) ^ 1); else K1_DMA_B(1, _tt, (b) ^ 1); } \
        else { const int _tt = ((t) + 2 < 16) ? (t) + 2 : 15; if (wr == 0) K1_DMA_B(0, _tt, (b)); else K1_DMA_A(0, _tt, (b)); } \
        WAIT_V(9); WAIT_L(0); SCHED(); BAR(); SCHED(); \
        __builtin_amdgcn_s_setprio(1); \
        _Pragma("unroll") for (int _m = 0; _m < 8; ++_m) _Pragma("unroll") for (int _n = 0; _n < 5; ++_n) \
            acc[_m][_n] = __builtin_amdgcn_mfma_f32_16x16x32_bf16(bfg[_n], af[_m], acc[_m][_n], 0, 0, 0); \
        __builtin_amdgcn_s_setprio(0); \
        SCHED(); BAR(); SCHED(); } while (0)

    for (int t = 0; t < 16; t += 2) {
        K1_SEG(0, 0, t); K1_SEG(0, 1, t); K1_SEG(1, 0, t + 1); K1_SEG(1, 1, t + 1);
    }
    if (wr == 0) BAR();
    STAMP(0, 2);

    const bool roleP = type1 || wc < 2;
    const int row0 = rb * 256 + wr * 128 + fr;
    bf16_t* Ap = (bf16_t*)(ws + WS_A);
    bf16_t* Qb = (bf16_t*)(ws + WS_Q); bf16_t* Kb = (bf16_t*)(ws + WS_K); bf16_t* Vb = (bf16_t*)(ws + WS_V);
    const bool odd = fq & 1;
    {
        const int gam = type1 ? t1 : 24 + 3 * t2;
        const int col = 64 * gam + 16 * wc + 4 * fq;
        const int part = col >> 10, head = (col & 1023) >> 5, d = col & 31;
        const float sc = part == 0 ? QSCALE : 1.0f;
#pragma unroll
        for (int m = 0; m < 8; ++m) {
            const int row = row0 + 16 * m;
            const f32x4 v = acc[m][4] * sc;
            u32x2 w; w.x = cvtpk(v[0], v[1]); w.y = cvtpk(v[2], v[3]);
            bf16_t* dst;
            if (part == 1) dst = Kb + (size_t)head * 65536 + (row >> 6) * 2048 + (d >> 3) * 512 + (row & 63) * 8 + (d & 7);
            else dst = (part == 0 ? Qb : Vb) + ((size_t)head * SEQ + row) * 32 + d;
            *(u32x2*)dst = w;
        }
    }
    if (roleP) {
        const int hcol = (type1 ? 128 * t1 : 3072 + 64 * t2) + 32 * wc + 8 * fq;
#pragma unroll
        for (int m = 0; m < 8; ++m) {
            const int row = row0 + 16 * m;
            float h0[4], h1[4];
#pragma unroll
            for (int j = 0; j < 4; ++j) { h0[j] = gelu_tanh(acc[m][0][j]) * acc[m][2][j]; h1[j] = gelu_tanh(acc[m][1][j]) * acc[m][3][j]; }
            const unsigned a0 = cvtpk(h0[0], h0[1]), a1 = cvtpk(h0[2], h0[3]), b0 = cvtpk(h1[0], h1[1]), b1 = cvtpk(h1[2], h1[3]);
            u32x4 w; w.x = odd ? b0 : a0; w.y = odd ? b1 : a1; w.z = odd ? a0 : b0; w.w = odd ? a1 : b1;
            *(u32x4*)(Ap + (size_t)row * KTOT + hcol) = w;
        }
    } else {
#pragma unroll
        for (int pr = 0; pr < 2; ++pr) {
            const int gam = 24 + 3 * t2 + (wc - 1);
            const int col = 64 * gam + 32 * pr + 8 * fq;
            const int part = col >> 10, head = (col & 1023) >> 5, d = col & 31;
            const float sc = part == 0 ? QSCALE : 1.0f;
#pragma unroll
            for (int m = 0; m < 8; ++m) {
                const int row = row0 + 16 * m;
                const f32x4 v0 = acc[m][2 * pr] * sc, v1 = acc[m][2 * pr + 1] * sc;
                const unsigned a0 = cvtpk(v0[0], v0[1]), a1 = cvtpk(v0[2], v0[3]), b0 = cvtpk(v1[0], v1[1]), b1 = cvtpk(v1[2], v1[3]);
                u32x4 w; w.x = odd ? b0 : a0; w.y = odd ? b1 : a1; w.z = odd ? a0 : b0; w.w = odd ? a1 : b1;
                bf16_t* dst;
                if (part == 1) dst = Kb + (size_t)head * 65536 + (row >> 6) * 2048 + (d >> 3) * 512 + (row & 63) * 8;
                else dst = (part == 0 ? Qb : Vb) + ((size_t)head * SEQ + row) * 32 + d;
                *(u32x4*)dst = w;
            }
        }
    }
    STAMP(0, 3);
    WAIT_V(0);
    STAMP(0, 4);
#undef K1_SEG
#undef K1_DMA_A
#undef K1_DMA_B
}

constexpr int K3_STG = 49152;
constexpr int K23_SCR = 3 * K3_STG;
constexpr int K23_LDS = K23_SCR + 1024;
#define K2_RING(j) (K3_STG + (((j) + 1) % 3) * 32768)
__device__ __forceinline__ int crow(int r, int hi) { return (r & 3) + 8 * (r >> 2) + 4 * hi; }
#define MFMA32(a, b, c) __builtin_amdgcn_mfma_f32_32x32x16_bf16(a, b, c, 0, 0, 0)
#define MFMA16(a, b, c) __builtin_amdgcn_mfma_f32_16x16x32_bf16(a, b, c, 0, 0, 0)
#define MX3(a, b, c) __builtin_fmaxf(__builtin_fmaxf((a), (b)), (c))
#define PIN(x) asm volatile("" : "+v"(x))
#define EX2(v) __builtin_amdgcn_exp2f(v)
__device__ __forceinline__ void glds4_sc1(const char* sbase, unsigned voff, unsigned ldsaddr) {
    asm volatile("s_mov_b32 m0, %2\n\ts_nop 0\n\tglobal_load_lds_dword %0, %1 sc1" :: "v"(voff), "s"(sbase), "s"(ldsaddr) : "memory"); }
__device__ __forceinline__ void glds4(const char* sbase, unsigned voff, unsigned ldsaddr) {
    asm volatile("s_mov_b32 m0, %2\n\ts_nop 0\n\tglobal_load_lds_dword %0, %1" :: "v"(voff), "s"(sbase), "s"(ldsaddr) : "memory"); }

__global__ __launch_bounds__(768, 3) void k23(const bf16_t* __restrict__ wo, const bf16_t* __restrict__ wd, unsigned char* __restrict__ ws, bf16_t* __restrict__ out) {
    extern __shared__ __attribute__((aligned(16))) char smem[];
    const unsigned lds = (unsigned)(uintptr_t)(LAS char*)smem;
    const int tid = threadIdx.x, wid = __builtin_amdgcn_readfirstlane(tid >> 6), lane = tid & 63;
    const int bid = blockIdx.x, xcd = bid & 7, idx = bid >> 3;
    unsigned* flags = (unsigned*)(ws + WS_FLAG);
    const int kq = xcd >> 1, mb = (xcd & 1) * 4 + (idx >> 3), nb = idx & 7;
    const int head = xcd * 4 + (idx & 3), qb = idx >> 2;
    LAS unsigned* fl = (LAS unsigned*)(uintptr_t)(lds + K23_SCR);

    if (wid >= 8) {
        const int L = wid - 8;
        const int lr = lane >> 3, ls = lane & 7;
        const char* Kg = (const char*)(ws + WS_K) + (size_t)head * 131072;
        const char* Vg = (const char*)(ws + WS_V) + (size_t)head * 131072;
        const unsigned voff = (unsigned)(L * 4096 + lane * 16);
#define K2_DMA(j) do { const char* _k = Kg + (size_t)(j) * 16384; const char* _v = Vg + (size_t)(j) * 16384; const unsigned _d = lds + K2_RING(j) + L * 4096; \
        _Pragma("unroll") for (int _i = 0; _i < 4; ++_i) glds16(_k + _i * 1024, voff, _d + _i * 1024); \
        _Pragma("unroll") for (int _i = 0; _i < 4; ++_i) glds16(_v + _i * 1024, voff, _d + 16384 + _i * 1024); } while (0)
        const unsigned voffA0 = (unsigned)(lr * (KTOT * 2) + ((ls ^ ((lane >> 4) & 7)) * 16)), voffA1 = (unsigned)(lr * (KTOT * 2) + ((ls ^ ((4 + (lane >> 4)) & 7)) * 16));
        const unsigned voffB0 = (unsigned)(lr * (DM * 2) + ((ls ^ (3 * ((lane >> 4) & 1))) * 16)), voffB1 = (unsigned)(lr * (DM * 2) + ((ls ^ (3 * ((lane >> 4) & 1) + 4)) * 16));
        const char* Ag = (const char*)(ws + WS_A) + (size_t)(mb * 256 + L * 32) * KTOT * 2;
#define K3_DMA_A(tt, h) do { const int _t = (tt) < 18 ? (tt) : 17; const unsigned _d = lds + ((tt) % 3) * K3_STG + (h) * 16384 + L * 4096; \
        const char* _s = Ag + (_t < 14 ? (14 * kq + _t) * 128 : FF * 2 + (4 * kq + _t - 14) * 128) + (size_t)((h) * 128) * KTOT * 2; \
        glds16(_s, voffA0, _d); glds16(_s + (size_t)8 * KTOT * 2, voffA1, _d + 1024); glds16(_s + (size_t)16 * KTOT * 2, voffA0, _d + 2048); glds16(_s + (size_t)24 * KTOT * 2, voffA1, _d + 3072); } while (0)
#define K3_DMA_B(tt, g) do { const int _t = (tt) < 18 ? (tt) : 17; const unsigned _d = lds + ((tt) % 3) * K3_STG + 32768 + (g) * 8192 + L * 2048; \
        const char* _w = (_t < 14 ? (const char*)wd + (size_t)(14 * kq + _t) * 64 * DM * 2 : (const char*)wo + (size_t)(4 * kq + _t - 14) * 64 * DM * 2) + nb * 256 + (g) * 128 + (size_t)(L * 16) * DM * 2; \
        glds16(_w, voffB0, _d); glds16(_w + (size_t)8 * DM * 2, voffB1, _d + 1024); } while (0)
        STAMP(1, 0);
        K2_DMA(0); K2_DMA(1);
        WAIT_V(0); BAR();
#pragma unroll
        for (int j = 0; j < 8; ++j) {
            if (j >= 1 && j <= 3) { WAIT_V(1); BAR(); } else if (j == 4) { WAIT_V(12); BAR(); } else if (j == 5 || j == 6) { WAIT_V(0); BAR(); } else if (j == 7) { BAR(); }
            if (j + 2 < 8) K2_DMA(j + 2);
            if (j < 3) {
                const int ins = j * 4 + L;
                if (ins < 9) {
                    const int row0 = (idx * 9 + ins) * 4;
                    const char* base = row0 < 896 ? (const char*)wd + (size_t)(896 * kq + row0) * 2048 : (const char*)wo + (size_t)(256 * kq + row0 - 896) * 2048;
                    glds4(base, (unsigned)(lane * 128), lds + K23_SCR + 256);
                } else {
                    const int x = idx * 3 + (ins - 9);
                    const char* base = (const char*)(ws + WS_A) + (size_t)((xcd & 1) * 1024 + (x & 15) * 64) * (KTOT * 2) + (14 * kq + 2 + (x >> 4)) * 128;
                    glds4(base, (unsigned)(lane * (KTOT * 2)), lds + K23_SCR + 256);
                }
            }
            if (j == 3) { K3_DMA_A(0, 0); K3_DMA_B(0, 0); K3_DMA_B(0, 1); K3_DMA_A(0, 1); }
            if (j == 6) { K3_DMA_A(1, 0); K3_DMA_A(1, 1); }
            if (j == 7) { K3_DMA_B(1, 0); K3_DMA_B(1, 1); }
        }
        BAR(); BAR();
        const unsigned redo = __builtin_amdgcn_readfirstlane(fl[0]);
        if (redo) {
            WAIT_V(0); BAR();
            K2_DMA(0); K2_DMA(1);
            for (int j = 0; j < 8; ++j) {
                if (j < 7) WAIT_V(8); else WAIT_V(0);
                BAR();
                if (j + 2 < 8) K2_DMA(j + 2);
            }
            BAR();
            K3_DMA_A(1, 0); K3_DMA_B(1, 0); K3_DMA_B(1, 1); K3_DMA_A(1, 1);
        }
        WAIT_V(0); BAR();
#define K3_DMA_T(tt) do { K3_DMA_A(tt, 0); K3_DMA_B(tt, 0); K3_DMA_B(tt, 1); K3_DMA_A(tt, 1); } while (0)
        K3_DMA_T(2);
        int ost = 0;
        for (int t = 0; t < 18; ++t) {
            if (L == 0 && t >= 4 && t <= 11) {
                if (t == 8 || t == 10 || t == 11) {
                    WAIT_V(12);
                    if (ost == 0) {
                        if ((unsigned)__builtin_amdgcn_readfirstlane(fl[128]) >= 8u) ost = 2;
                        else if (t == 11) {
                            unsigned spins = 0;
                            while (__builtin_amdgcn_readfirstlane(__hip_atomic_load(flags + (256 + mb * 4 + kq) * 64, RLX_AGENT)) < 8u) { __builtin_amdgcn_s_sleep(1); if (++spins > (1u << 22)) break; }
                            ost = 2;
                        }
                        if (ost == 2 && t == 11) { __builtin_amdgcn_fence(__ATOMIC_ACQUIRE, "agent"); WAIT_V(0); ost = 1; }
                    } else if (t == 11 && ost == 3) WAIT_V(0);
                } else WAIT_V(13);
            } else WAIT_V(12);
            BAR();
            K3_DMA_T(t + 3);
            if (L == 0) {
                if (t == 3) { if (lane == 0) __hip_atomic_fetch_add(flags + (256 + qb * 4 + (xcd >> 1)) * 64, 1u, RLX_AGENT); }
                else if (t == 5 || (t == 8 && ost == 0)) glds4_sc1((const char*)(flags + (256 + mb * 4 + kq) * 64), 0u, lds + K23_SCR + 512);
                else if (ost == 2) { asm volatile("buffer_inv sc1" ::: "memory"); ost = (t == 10) ? 3 : 1; }
            }
        }
#undef K3_DMA_T
        WAIT_V(0);
        BAR(); BAR();
        return;
#undef K2_DMA
#undef K3_DMA_A
#undef K3_DMA_B
    }

    float omg[4];
    { const float* gv = (const float*)(ws + WS_G) + mb * 256 + (wid >> 2) * 128 + ((wid & 3) >> 1) * 64 + (lane & 15);
#pragma unroll
      for (int m = 0; m < 4; ++m) omg[m] = 1.0f - gv[16 * m]; }

    {
        const int r32 = lane & 31, hi = lane >> 5;
        const bf16_t* Qg = (const bf16_t*)(ws + WS_Q) + ((size_t)head * SEQ + qb * 256 + wid * 32) * 32;
        bf16x8 qf0 = *(const bf16x8*)(Qg + r32 * 32 + hi * 8);
        bf16x8 qf1 = *(const bf16x8*)(Qg + r32 * 32 + 16 + hi * 8);
        float gval = ((const float*)(ws + WS_G))[qb * 256 + wid * 32 + r32];
        bf16x8 ones;
        { const short o1 = (((lane >> 4) & 1) == (((lane & 15) >> 2) & 1)) ? (short)0x3F80 : (short)0;
#pragma unroll
          for (int j = 0; j < 8; ++j) ones[j] = o1; }
        const unsigned koff = lds + hi * 1024 + r32 * 16;
        const unsigned voffr = lds + 16384 + ((lane >> 4) & 1) * 32 + (lane & 3) * 8 + (4 * hi + ((lane & 15) >> 2)) * 64;
        const unsigned koff0 = koff + K2_RING(0);
        f32x16 o = {}; f32x4 lsum = {0.f, 0.f, 0.f, 0.f};
        asm volatile("" : "+v"(omg[0]), "+v"(omg[1]), "+v"(omg[2]), "+v"(omg[3]), "+v"(gval), "+v"(qf0), "+v"(qf1));
        BAR();
        STAMP(1, 1);
        {
            f32x16 SA, SB, negm;
            u32x4 WA0, WA1, WB0 = {0u, 0u, 0u, 0u}, WB1 = {0u, 0u, 0u, 0u};
            bf16x8 kf0, kf1, vf0 = {}, vf1 = {};
            kf0 = *(LAS const bf16x8*)(LDSP(koff0)); kf1 = *(LAS const bf16x8*)(LDSP(koff0) + 2048);
            { f32x16 z = {}; SA = MFMA32(kf0, qf0, z); SA = MFMA32(kf1, qf1, SA); }
            kf0 = *(LAS const bf16x8*)(LDSP(koff0) + 512); kf1 = *(LAS const bf16x8*)(LDSP(koff0) + 2048 + 512);
            {
                float a = MX3(SA[0], SA[1], SA[2]), b = MX3(SA[3], SA[4], SA[5]);
                a = MX3(a, SA[6], SA[7]); b = MX3(b, SA[8], SA[9]); a = MX3(a, SA[10], SA[11]); b = MX3(b, SA[12], SA[13]); a = MX3(a, SA[14], SA[15]);
                float rm = __builtin_fmaxf(a, b);
                auto rr = __builtin_amdgcn_permlane32_swap(__float_as_uint(rm), __float_as_uint(rm), false, false); rm = __builtin_fmaxf(__uint_as_float(rr[0]), __uint_as_float(rr[1]));
#pragma unroll
                for (int r = 0; r < 16; ++r) { SA[r] -= rm; negm[r] = -rm; }
                PIN(negm);
            }
#define K2_STEP(C, N, WP0, WP1, WC0, WC1, KADDR, VADDR) do { \
            N = MFMA32(kf0, qf0, negm); \
            C[0] = EX2(C[0]); C[1] = EX2(C[1]); C[2] = EX2(C[2]); C[3] = EX2(C[3]); PIN(C); SCHED(); \
            o = MFMA32(vf0, __builtin_bit_cast(bf16x8, WP0), o); \
            C[4] = EX2(C[4]); C[5] = EX2(C[5]); C[6] = EX2(C[6]); C[7] = EX2(C[7]); PIN(C); SCHED(); \
            lsum = MFMA16(ones, __builtin_bit_cast(bf16x8, WP0), lsum); \
            WC0[0] = cvtpk(C[0], C[1]); WC0[1] = cvtpk(C[2], C[3]); WC0[2] = cvtpk(C[4], C[5]); WC0[3] = cvtpk(C[6], C[7]); PIN(WC0); SCHED(); \
            N = MFMA32(kf1, qf1, N); \
            C[8] = EX2(C[8]); C[9] = EX2(C[9]); C[10] = EX2(C[10]); C[11] = EX2(C[11]); PIN(C); SCHED(); \
            kf0 = *(LAS const bf16x8*)(KADDR); kf1 = *(LAS const bf16x8*)((KADDR) + 2048); SCHED(); \
            o = MFMA32(vf1, __builtin_bit_cast(bf16x8, WP1), o); \
            C[12] = EX2(C[12]); C[13] = EX2(C[13]); C[14] = EX2(C[14]); C[15] = EX2(C[15]); PIN(C); SCHED(); \
            lsum = MFMA16(ones, __builtin_bit_cast(bf16x8, WP1), lsum); \
            vf0 = cat8(trread(VADDR), trread((VADDR) + 512)); vf1 = cat8(trread((VADDR) + 1024), trread((VADDR) + 1536)); \
            WC1[0] = cvtpk(C[8], C[9]); WC1[1] = cvtpk(C[10], C[11]); WC1[2] = cvtpk(C[12], C[13]); WC1[3] = cvtpk(C[14], C[15]); PIN(WC1); SCHED(); \
            } while (0)
            for (int j = 0; j < 8; ++j) {
                if (j > 0) { WAIT_L(0); BAR(); }
                LAS const char* kc = LDSP(koff + K2_RING(j));
                LAS const char* kn = LDSP(koff + K2_RING(j + 1));
                LAS const char* vc = LDSP(voffr + K2_RING(j));
                K2_STEP(SA, SB, WB0, WB1, WA0, WA1, kc + 1 * 4096, vc + 0 * 2048);
                K2_STEP(SB, SA, WA0, WA1, WB0, WB1, kc + 1 * 4096 + 512, vc + 1 * 2048);
                K2_STEP(SA, SB, WB0, WB1, WA0, WA1, kc + 2 * 4096, vc + 2 * 2048);
                K2_STEP(SB, SA, WA0, WA1, WB0, WB1, kc + 2 * 4096 + 512, vc + 3 * 2048);
                K2_STEP(SA, SB, WB0, WB1, WA0, WA1, kc + 3 * 4096, vc + 4 * 2048);
                K2_STEP(SB, SA, WA0, WA1, WB0, WB1, kc + 3 * 4096 + 512, vc + 5 * 2048);
                K2_STEP(SA, SB, WB0, WB1, WA0, WA1, kn, vc + 6 * 2048);
                K2_STEP(SB, SA, WA0, WA1, WB0, WB1, kn + 512, vc + 7 * 2048);
            }
            o = MFMA32(vf0, __builtin_bit_cast(bf16x8, WB0), o); lsum = MFMA16(ones, __builtin_bit_cast(bf16x8, WB0), lsum);
            o = MFMA32(vf1, __builtin_bit_cast(bf16x8, WB1), o); lsum = MFMA16(ones, __builtin_bit_cast(bf16x8, WB1), lsum);
#undef K2_STEP
        }
        STAMP(1, 2);
        {
            const bool bad = !(lsum[0] <= 1.0e9f);
            if (tid == 0) fl[0] = 0u;
            WAIT_L(0); BAR();
            if (__any(bad) && lane == 0) fl[0] = 1u;
            WAIT_L(0); BAR();
            const unsigned redo = __builtin_amdgcn_readfirstlane(fl[0]);
            if (redo) {
                WAIT_L(0); BAR();
                o = (f32x16){}; lsum = (f32x4){0.f, 0.f, 0.f, 0.f};
                float mhat = 0.f; f32x16 negm = {};
                bool first = true;
                for (int j = 0; j < 8; ++j) {
                    WAIT_L(0); BAR();
                    LAS const char* kb = LDSP(koff + K2_RING(j));
                    LAS const char* vb = LDSP(voffr + K2_RING(j));
#pragma unroll 1
                    for (int tt = 0; tt < 4; ++tt) {
                        LAS const char* kp = kb + tt * 4096;
                        LAS const char* vp = vb + tt * 4096;
                        const bf16x8 k00 = *(LAS const bf16x8*)(kp), k01 = *(LAS const bf16x8*)(kp + 2048), k10 = *(LAS const bf16x8*)(kp + 512), k11 = *(LAS const bf16x8*)(kp + 2048 + 512);
                        f32x16 p0 = MFMA32(k00, qf0, negm); p0 = MFMA32(k01, qf1, p0);
                        f32x16 p1 = MFMA32(k10, qf0, negm); p1 = MFMA32(k11, qf1, p1);
                        float a = MX3(p0[0], p0[1], p1[0]), b = MX3(p0[2], p0[3], p1[1]); a = MX3(a, p1[2], p1[3]);
#pragma unroll
                        for (int r = 4; r < 16; r += 4) { a = MX3(a, p0[r], p0[r + 1]); b = MX3(b, p0[r + 2], p0[r + 3]); a = MX3(a, p1[r], p1[r + 1]); b = MX3(b, p1[r + 2], p1[r + 3]); }
                        float rm = __builtin_fmaxf(a, b);
                        { auto rr = __builtin_amdgcn_permlane32_swap(__float_as_uint(rm), __float_as_uint(rm), false, false); rm = __builtin_fmaxf(__uint_as_float(rr[0]), __uint_as_float(rr[1])); }
                        {
                            const float dl = first ? rm : __builtin_fmaxf(rm, 0.f);
                            mhat += dl;
#pragma unroll
                            for (int r = 0; r < 16; ++r) { p0[r] -= dl; p1[r] -= dl; negm[r] = -mhat; }
                            if (!first) { const float f = EX2(-dl); o = o * f; lsum = lsum * f; }
                            first = false;
                        }
#pragma unroll
                        for (int r = 0; r < 16; ++r) { p0[r] = EX2(p0[r]); p1[r] = EX2(p1[r]); }
                        const u32x4 w0 = {cvtpk(p0[0], p0[1]), cvtpk(p0[2], p0[3]), cvtpk(p0[4], p0[5]), cvtpk(p0[6], p0[7])};
                        const u32x4 w1 = {cvtpk(p0[8], p0[9]), cvtpk(p0[10], p0[11]), cvtpk(p0[12], p0[13]), cvtpk(p0[14], p0[15])};
                        const u32x4 w2 = {cvtpk(p1[0], p1[1]), cvtpk(p1[2], p1[3]), cvtpk(p1[4], p1[5]), cvtpk(p1[6], p1[7])};
                        const u32x4 w3 = {cvtpk(p1[8], p1[9]), cvtpk(p1[10], p1[11]), cvtpk(p1[12], p1[13]), cvtpk(p1[14], p1[15])};
                        const bf16x8 v0 = cat8(trread(vp), trread(vp + 512)), v1 = cat8(trread(vp + 1024), trread(vp + 1536));
                        const bf16x8 v2 = cat8(trread(vp + 2048), trread(vp + 2560)), v3 = cat8(trread(vp + 3072), trread(vp + 3584));
                        const bf16x8 f0 = __builtin_bit_cast(bf16x8, w0), f1 = __builtin_bit_cast(bf16x8, w1), f2 = __builtin_bit_cast(bf16x8, w2), f3 = __builtin_bit_cast(bf16x8, w3);
                        o = MFMA32(v0, f0, o); lsum = MFMA16(ones, f0, lsum);
                        o = MFMA32(v1, f1, o); lsum = MFMA16(ones, f1, lsum);
                        o = MFMA32(v2, f2, o); lsum = MFMA16(ones, f2, lsum);
                        o = MFMA32(v3, f3, o); lsum = MFMA16(ones, f3, lsum);
                    }
                }
                WAIT_L(0); BAR();
            }
        }
        {
            const float sc = gval * __builtin_amdgcn_rcpf(lsum[0]);
            unsigned w[8];
#pragma unroll
            for (int g = 0; g < 4; ++g) { w[2 * g] = cvtpk(o[4 * g] * sc, o[4 * g + 1] * sc); w[2 * g + 1] = cvtpk(o[4 * g + 2] * sc, o[4 * g + 3] * sc); }
            const __amdgpu_buffer_rsrc_t rs = __builtin_amdgcn_make_buffer_rsrc((void*)(ws + WS_A), (short)0, (int)(SEQ * KTOT * 2), 0x00020000);
            const unsigned ob = (unsigned)(((qb * 256 + wid * 32 + r32) * KTOT + FF + head * 32 + 8 * hi) * 2);
#pragma unroll
            for (int pr = 0; pr < 2; ++pr) {
                auto r0 = __builtin_amdgcn_permlane32_swap(w[4 * pr], w[4 * pr + 2], false, false);
                auto r1 = __builtin_amdgcn_permlane32_swap(w[4 * pr + 1], w[4 * pr + 3], false, false);
                const u32x4 v = {r0[0], r1[0], r0[1], r1[1]};
                __builtin_amdgcn_raw_buffer_store_b128(v, rs, ob + pr * 32, 0, 16);
            }
            STAMP(1, 3);
            WAIT_L(0); BAR();
            STAMP(1, 4);
        }
    }

    {
        const int G = wid >> 2, j4 = wid & 3, wm = j4 >> 1, wn = j4 & 1, fr = lane & 15, fq = lane >> 4;
        const int arow = G * 128 + wm * 64 + fr;
        const unsigned aoff0 = lds + arow * 128 + ((fq ^ (fr >> 1)) * 16);
        const unsigned aoff1 = lds + arow * 128 + (((fq ^ (fr >> 1)) ^ 4) * 16);
        const int q4 = (lane >> 2) & 3, p4 = lane & 3;
        const int sR = 3 * ((q4 >> 1) & 1) + 4 * (fq & 1);
        const int krow = (8 * fq + q4) * 128;
        const unsigned boff00 = lds + 32768 + wn * 8192 + krow + ((p4 ^ sR) * 16) + (p4 & 1) * 8;
        const unsigned boff01 = lds + 32768 + wn * 8192 + krow + ((p4 ^ sR) * 16) + ((p4 & 1) ^ 1) * 8;
        const unsigned boff10 = lds + 32768 + wn * 8192 + krow + (((4 + p4) ^ sR) * 16) + (p4 & 1) * 8;
        const unsigned boff11 = lds + 32768 + wn * 8192 + krow + (((4 + p4) ^ sR) * 16) + ((p4 & 1) ^ 1) * 8;
        f32x4 acc[4][4];
#pragma unroll
        for (int m = 0; m < 4; ++m)
#pragma unroll
            for (int n = 0; n < 4; ++n) acc[m][n] = (f32x4){0.f, 0.f, 0.f, 0.f};
        unsigned fts = 0;
        STAMP(2, 0);
        STAMP(2, 1);
#define K3_READ(F_A, F_B, so_, ks_) do { \
            F_B[0] = cat8(trread(LDSP(boff00 + (so_)) + (ks_) * 4096), trread(LDSP(boff00 + (so_)) + (ks_) * 4096 + 512)); \
            F_A[0] = *(LAS const bf16x8*)(LDSP(((ks_) ? aoff1 : aoff0) + (so_))); \
            F_B[1] = cat8(trread(LDSP(boff01 + (so_)) + (ks_) * 4096), trread(LDSP(boff01 + (so_)) + (ks_) * 4096 + 512)); \
            F_A[1] = *(LAS const bf16x8*)(LDSP(((ks_) ? aoff1 : aoff0) + (so_)) + 2048); \
            F_B[2] = cat8(trread(LDSP(boff10 + (so_)) + (ks_) * 4096), trread(LDSP(boff10 + (so_)) + (ks_) * 4096 + 512)); \
            F_A[2] = *(LAS const bf16x8*)(LDSP(((ks_) ? aoff1 : aoff0) + (so_)) + 4096); \
            F_B[3] = cat8(trread(LDSP(boff11 + (so_)) + (ks_) * 4096), trread(LDSP(boff11 + (so_)) + (ks_) * 4096 + 512)); \
            F_A[3] = *(LAS const bf16x8*)(LDSP(((ks_) ? aoff1 : aoff0) + (so_)) + 6144); } while (0)
#define K3_RB(dst, boff, so_, ks_) dst = cat8(trread(LDSP((boff) + (so_)) + (ks_) * 4096), trread(LDSP((boff) + (so_)) + (ks_) * 4096 + 512))
#define K3_RA(dst, so_, ks_, m_) dst = *(LAS const bf16x8*)(LDSP(((ks_) ? aoff1 : aoff0) + (so_)) + (m_) * 2048)
#define K3_M(CA, CB, m_, n_) acc[m_][n_] = MFMA16(CB[n_], CA[m_], acc[m_][n_])
#define K3_STEP(CA, CB, NA, NB, so_, ks_) do { \
            K3_M(CA, CB, 0, 0); K3_RA(NA[0], so_, ks_, 0); SCHED(); \
            K3_M(CA, CB, 0, 1); K3_RB(NB[0], boff00, so_, ks_); SCHED(); \
            K3_M(CA, CB, 0, 2); SCHED(); \
            K3_M(CA, CB, 0, 3); K3_RB(NB[1], boff01, so_, ks_); SCHED(); \
            K3_M(CA, CB, 1, 0); SCHED(); \
            K3_M(CA, CB, 1, 1); K3_RB(NB[2], boff10, so_, ks_); SCHED(); \
            K3_M(CA, CB, 1, 2); SCHED(); \
            K3_M(CA, CB, 1, 3); K3_RB(NB[3], boff11, so_, ks_); SCHED(); \
            K3_M(CA, CB, 2, 0); SCHED(); \
            K3_M(CA, CB, 2, 1); K3_RA(NA[1], so_, ks_, 1); SCHED(); \
            K3_M(CA, CB, 2, 2); K3_RA(NA[2], so_, ks_, 2); SCHED(); \
            K3_M(CA, CB, 2, 3); K3_RA(NA[3], so_, ks_, 3); SCHED(); \
            K3_M(CA, CB, 3, 0); K3_M(CA, CB, 3, 1); K3_M(CA, CB, 3, 2); K3_M(CA, CB, 3, 3); SCHED(); } while (0)
        bf16x8 a0[4], b0[4], a1[4], b1[4];
        K3_READ(a0, b0, 0u, 0);
        for (int t = 0; t < 18; ++t) {
            const unsigned so = (t % 3) * K3_STG, sn = ((t + 1) % 3) * K3_STG;
            if (t == 14) {
#pragma unroll
                for (int m = 0; m < 4; ++m)
#pragma unroll
                    for (int n = 0; n < 4; ++n) acc[m][n] = acc[m][n] * omg[m];
            }
            SCHED();
            K3_STEP(a0, b0, a1, b1, so, 1);
            if (t == 3) WAIT_V(0);
            WAIT_L(0); SCHED(); BAR(); SCHED();
            K3_STEP(a1, b1, a0, b0, sn, 0);
        }
#undef K3_STEP
#undef K3_M
#undef K3_RA
#undef K3_RB
#undef K3_READ
        STAMP(2, 2);
        {
            const __amdgpu_buffer_rsrc_t rs = __builtin_amdgcn_make_buffer_rsrc((void*)(ws + WS_P), (short)0, (int)(256 * 131072), 0x00020000);
            const unsigned so = (unsigned)(bid * 131072 + wid * 4096 + lane * 16);
            f32x4 fin[4];
#define K3_SEND(q) do { _Pragma("unroll") for (int n = 0; n < 4; ++n) __builtin_amdgcn_raw_buffer_store_b128(__builtin_bit_cast(u32x4, acc[q][n]), rs, so + (q) * 32768 + n * 1024, 0, 16); } while (0)
#define K3_KEEP(q) do { _Pragma("unroll") for (int n = 0; n < 4; ++n) fin[n] = acc[q][n]; } while (0)
            if (kq == 0) { K3_SEND(1); K3_SEND(2); K3_SEND(3); K3_KEEP(0); }
            else if (kq == 1) { K3_SEND(0); K3_SEND(2); K3_SEND(3); K3_KEEP(1); }
            else if (kq == 2) { K3_SEND(0); K3_SEND(1); K3_SEND(3); K3_KEEP(2); }
            else { K3_SEND(0); K3_SEND(1); K3_SEND(2); K3_KEEP(3); }
#undef K3_SEND
#undef K3_KEEP
            STAMP(2, 3);
            WAIT_V(0); BAR();
            STAMP(2, 4);
            if (wid == 0) {
                if (lane == 0) __hip_atomic_store(flags + bid * 64, 1u, RLX_AGENT);
                const int pb = idx * 8 + 2 * (lane & 3) + (xcd & 1);
                unsigned spins = 0;
                for (;;) {
                    const unsigned v = __hip_atomic_load(flags + pb * 64, RLX_AGENT);
                    if (__all(v != 0u)) break;
                    __builtin_amdgcn_s_sleep(1); if (++spins > (1u << 22)) break;
                }
            }
            BAR();
            STAMP(2, 5);
            f32x4 rv[3][4];
#pragma unroll
            for (int i = 0; i < 3; ++i) {
                const int q = (kq + 1 + i) & 3;
                const unsigned po = (unsigned)((idx * 8 + 2 * q + (xcd & 1)) * 131072 + kq * 32768 + wid * 4096 + lane * 16);
#pragma unroll
                for (int n = 0; n < 4; ++n) rv[i][n] = __builtin_bit_cast(f32x4, __builtin_amdgcn_raw_buffer_load_b128(rs, po + n * 1024, 0, 16));
            }
#pragma unroll
            for (int n = 0; n < 4; ++n) fin[n] += (rv[0][n] + rv[1][n]) + rv[2][n];
            const int row = mb * 256 + G * 128 + wm * 64 + kq * 16 + fr;
            const bool odd = fq & 1;
#pragma unroll
            for (int pr = 0; pr < 2; ++pr) {
                const f32x4 v0 = fin[2 * pr], v1 = fin[2 * pr + 1];
                const unsigned a0 = cvtpk(v0[0], v0[1]), a1 = cvtpk(v0[2], v0[3]), b0 = cvtpk(v1[0], v1[1]), b1 = cvtpk(v1[2], v1[3]);
                u32x4 w; w.x = odd ? b0 : a0; w.y = odd ? b1 : a1; w.z = odd ? a0 : b0; w.w = odd ? a1 : b1;
                *(u32x4*)(out + (size_t)row * DM + nb * 128 + wn * 64 + pr * 32 + 8 * fq) = w;
            }
        }
        STAMP(2, 6);
        WAIT_V(0);
        STAMP(2, 7);
    }
}

extern "C" void kernel_launch(void* const* d_in, const int* in_sizes, int n_in, void* d_out, int out_size, void* d_ws, size_t ws_size, hipStream_t stream) {
    const bf16_t* x = (const bf16_t*)d_in[0];
    const bf16_t* wqkv = (const bf16_t*)d_in[1];
    const bf16_t* wo = (const bf16_t*)d_in[2];
    const bf16_t* wg = (const bf16_t*)d_in[4];
    const bf16_t* wu = (const bf16_t*)d_in[5];
    const bf16_t* wd = (const bf16_t*)d_in[6];
    const bf16_t* gw = (const bf16_t*)d_in[7];
    unsigned char* ws = (unsigned char*)d_ws;
    (void)hipFuncSetAttribute((const void*)k1_gemm, hipFuncAttributeMaxDynamicSharedMemorySize, K1_LDS);
    (void)hipFuncSetAttribute((const void*)k23, hipFuncAttributeMaxDynamicSharedMemorySize, K23_LDS);
    k1_gemm<<<256, 512, K1_LDS, stream>>>(x, wqkv, wg, wu, gw, ws);
    k23<<<256, 768, K23_LDS, stream>>>(wo, wd, ws, (bf16_t*)d_out);
}
```

```cpp
#include <hip/hip_runtime.h>
#include <hip/hip_bf16.h>
#include <stdint.h>

#define LAS __attribute__((address_space(3)))
typedef unsigned short bf16_t;
typedef short bf16x8 __attribute__((ext_vector_type(8)));
typedef short s16x4 __attribute__((ext_vector_type(4)));
typedef short v4i16_t __attribute__((ext_vector_type(4)));
typedef float f32x2 __attribute__((ext_vector_type(2)));
typedef float f32x4 __attribute__((ext_vector_type(4)));
typedef float f32x16 __attribute__((ext_vector_type(16)));
typedef unsigned u32x2 __attribute__((ext_vector_type(2)));
typedef unsigned u32x4 __attribute__((ext_vector_type(4)));
typedef __bf16 bf16x2_t __attribute__((ext_vector_type(2)));

constexpr int SEQ = 2048, DM = 1024, NQKV = 3072, FF = 3584, NH = 32, DH = 32;
constexpr int KTOT = FF + DM;
constexpr size_t WS_FLAG = 0;
constexpr size_t WS_G = 131072;
constexpr size_t WS_XC = 262144;
constexpr size_t WS_Q = 1u << 20;
constexpr size_t WS_K = WS_Q + (4u << 20);
constexpr size_t WS_V = WS_K + (4u << 20);
constexpr size_t WS_A = WS_V + (4u << 20);
constexpr size_t WS_P = WS_A + (20u << 20);

#define WAIT_V(n) asm volatile("s_waitcnt vmcnt(" #n ")" ::: "memory")
#define WAIT_L(n) asm volatile("s_waitcnt lgkmcnt(" #n ")" ::: "memory")
#define BAR() asm volatile("s_barrier" ::: "memory")
#define SCHED() __builtin_amdgcn_sched_barrier(0)
#define RLX_AGENT __ATOMIC_RELAXED, __HIP_MEMORY_SCOPE_AGENT
#define FINE 0
#define LSTAMP(i) do {} while (0)
#define STAMP(kern, i) do {} while (0)
#define FSTAMP(cond, i) do {} while (0)

__device__ __forceinline__ unsigned cvtpk(float lo, float hi) { f32x2 v = {lo, hi}; bf16x2_t b = __builtin_convertvector(v, bf16x2_t); return __builtin_bit_cast(unsigned, b); }
__device__ __forceinline__ float bf2f(unsigned short b) { return __uint_as_float(((unsigned)b) << 16); }
__device__ __forceinline__ s16x4 trread(LAS const char* p) { return __builtin_bit_cast(s16x4, __builtin_amdgcn_ds_read_tr16_b64_v4i16((LAS v4i16_t*)p)); }
__device__ __forceinline__ bf16x8 cat8(s16x4 lo, s16x4 hi) { return (bf16x8){lo[0], lo[1], lo[2], lo[3], hi[0], hi[1], hi[2], hi[3]}; }
__device__ __forceinline__ void glds16(const char* sbase, unsigned voff, unsigned ldsaddr) {
    asm volatile("s_mov_b32 m0, %2\n\ts_nop 0\n\tglobal_load_lds_dwordx4 %0, %1" :: "v"(voff), "s"(sbase), "s"(ldsaddr) : "memory"); }
__device__ __forceinline__ void glds16_sc1(const char* sbase, unsigned voff, unsigned ldsaddr) {
    asm volatile("s_mov_b32 m0, %2\n\ts_nop 0\n\tglobal_load_lds_dwordx4 %0, %1 sc1" :: "v"(voff), "s"(sbase), "s"(ldsaddr) : "memory"); }
#define LDSP(a) ((LAS const char*)(uintptr_t)(unsigned)(a))
#define CODE_LABEL(name) asm volatile(name ":")
#define CODE_RANGE(base, len, tag, lbeg, lend) do { unsigned long long _pc; int _d; \
    asm volatile("s_getpc_b64 %0\n" tag ":\n\ts_mov_b32 %1, " lbeg "-" tag "\n\ts_mov_b32 %2, " lend "-" lbeg : "=s"(_pc), "=s"(_d), "=s"(len)); \
    base = (const char*)(_pc + (long long)_d); } while (0)
__device__ __forceinline__ float gelu_tanh(float x) {
    const float c0 = -2.0f * 0.7978845608028654f * 1.4426950408889634f, c1 = c0 * 0.044715f;
    const float z = x * __builtin_fmaf(x * x, c1, c0);
    const float e = __builtin_amdgcn_exp2f(z);
    return x * __builtin_amdgcn_rcpf(1.0f + e);
}

constexpr int K1_LDS = 2 * 32768 + 2 * 40960;
constexpr float QSCALE = 0.17677669529663687f * 1.4426950408889634f;

__global__ __launch_bounds__(512, 2) void k1_gemm(const bf16_t* __restrict__ x, const bf16_t* __restrict__ wqkv, const bf16_t* __restrict__ wg,
                                                  const bf16_t* __restrict__ wu, const bf16_t* __restrict__ gate_w, unsigned char* __restrict__ ws) {
    extern __shared__ __attribute__((aligned(16))) char smem[];
    const unsigned lds = (unsigned)(uintptr_t)(LAS char*)smem;
    const int tid = threadIdx.x, wid = __builtin_amdgcn_readfirstlane(tid >> 6), lane = tid & 63;
    const int wr = wid >> 2, wc = wid & 3, fr = lane & 15, fq = lane >> 4;
    const int bid = blockIdx.x, xcd = bid & 7, idx = bid >> 3, ctl = idx & 3, rb = idx >> 2;
    const bool type1 = ctl < 3;
    const int t1 = xcd * 3 + ctl, t2 = xcd;
    const char* gp0; const char* gp1; const char* gp2; const char* gp3; const char* gp4;
    if (type1) {
        gp0 = (const char*)(wg + 128 * t1); gp1 = (const char*)(wg + 128 * t1 + 64); gp2 = (const char*)(wu + 128 * t1); gp3 = (const char*)(wu + 128 * t1 + 64);
        gp4 = (const char*)(wqkv + 64 * t1);
    } else {
        gp0 = (const char*)(wg + 3072 + 64 * t2); gp1 = (const char*)(wu + 3072 + 64 * t2);
        gp2 = (const char*)(wqkv + 64 * (24 + 3 * t2)); gp3 = (const char*)(wqkv + 64 * (24 + 3 * t2 + 1)); gp4 = (const char*)(wqkv + 64 * (24 + 3 * t2 + 2));
    }
    const unsigned ld23 = type1 ? FF * 2 : NQKV * 2;
    const int lr = lane >> 3, ls = lane & 7;
    const unsigned voffA = (unsigned)((8 * wc + lr) * (DM * 2) + ((ls ^ ((4 * (wc & 1) + (lane >> 4)) & 7)) * 16));
    const int sB = 3 * ((lane >> 4) & 1) + 4 * (wc & 1);
    const unsigned voffG = (unsigned)((8 * wc + lr) * (FF * 2) + ((ls ^ sB) * 16));
    const unsigned voffQ = (unsigned)((8 * wc + lr) * (NQKV * 2) + ((ls ^ sB) * 16));
    const unsigned voff23 = type1 ? voffG : voffQ;
    const char* xa = (const char*)x + (size_t)rb * 256 * DM * 2;
#define K1_DMA_A(h, tt, bb) do { const char* _s = xa + (size_t)(h) * 128 * DM * 2 + (size_t)(tt) * 128; \
        _Pragma("unroll") for (int _u = 0; _u < 4; ++_u) glds16(_s + (size_t)_u * 32 * DM * 2, voffA, lds + (bb) * 32768 + ((h) * 128 + _u * 32 + wc * 8) * 128); } while (0)
#define K1_DMA_B(hb, tt, bb) do { const size_t _k = (size_t)(tt) * 64 + (hb) * 32; const unsigned _d = lds + 65536 + (bb) * 40960 + ((hb) * 32 + wc * 8) * 128; \
        glds16(gp0 + _k * (FF * 2), voffG, _d); glds16(gp1 + _k * (FF * 2), voffG, _d + 8192); \
        glds16(gp2 + _k * ld23, voff23, _d + 16384); glds16(gp3 + _k * ld23, voff23, _d + 24576); \
        glds16(gp4 + _k * (NQKV * 2), voffQ, _d + 32768); } while (0)

    STAMP(0, 0);
    if (wr == 0) { K1_DMA_B(0, 0, 0); K1_DMA_A(1, 0, 0); K1_DMA_B(0, 1, 1); }
    else         { K1_DMA_A(0, 0, 0); K1_DMA_B(1, 0, 0); K1_DMA_A(0, 1, 1); }

    {
        const int row = rb * 256 + (xcd * 4 + ctl) * 8 + wid;
        const u32x4* xr = (const u32x4*)(x + (size_t)row * DM) + lane * 2;
        const u32x4* gw = (const u32x4*)gate_w + lane * 2;
        const u32x4 a0 = xr[0], a1 = xr[1], b0 = gw[0], b1 = gw[1];
        float s = 0.f;
#pragma unroll
        for (int i = 0; i < 4; ++i) {
            s = __builtin_fmaf(__uint_as_float(a0[i] << 16), __uint_as_float(b0[i] << 16), s);
            s = __builtin_fmaf(__uint_as_float(a0[i] & 0xffff0000u), __uint_as_float(b0[i] & 0xffff0000u), s);
            s = __builtin_fmaf(__uint_as_float(a1[i] << 16), __uint_as_float(b1[i] << 16), s);
            s = __builtin_fmaf(__uint_as_float(a1[i] & 0xffff0000u), __uint_as_float(b1[i] & 0xffff0000u), s);
        }
#pragma unroll
        for (int o = 32; o >= 1; o >>= 1) s += __shfl_xor(s, o);
        if (lane == 0) ((float*)(ws + WS_G))[row] = 1.0f / (1.0f + __expf(-s));
        if (tid == 0) ((unsigned*)(ws + WS_FLAG))[bid * 64] = 0u;
        if (tid == 64 && bid < 32) ((unsigned*)(ws + WS_FLAG))[(256 + bid) * 64] = 0u;
        if (tid >= 128 && tid < 136) ((unsigned*)(ws + WS_XC))[(bid * 8 + (tid - 128)) * 32] = 0u;
    }

    const unsigned aoff0 = lds + (wr * 128 + fr) * 128 + ((fq ^ (fr >> 1)) * 16);
    const unsigned aoff1 = lds + (wr * 128 + fr) * 128 + (((fq ^ (fr >> 1)) ^ 4) * 16);
    const int q4 = (lane >> 2) & 3, p4 = lane & 3;
    const int sR = 3 * ((q4 >> 1) & 1) + 4 * (fq & 1);
    const int krow = (8 * fq + q4) * 128;
    int gA, cA, gB, cB, gS;
    if (type1) { gA = wc >> 1; cA = 4 * (wc & 1); gB = 2 + (wc >> 1); cB = cA; gS = 4; }
    else if (wc < 2) { gA = 0; cA = 4 * wc; gB = 1; cB = cA; gS = 2; }
    else { gA = wc + 1; cA = 0; gB = wc + 1; cB = 4; gS = 2; }
    const unsigned boffA0 = lds + 65536 + gA * 8192 + krow + (((cA + p4) ^ sR) * 16) + (p4 & 1) * 8;
    const unsigned boffA1 = lds + 65536 + gA * 8192 + krow + (((cA + p4) ^ sR) * 16) + ((p4 & 1) ^ 1) * 8;
    const unsigned boffB0 = lds + 65536 + gB * 8192 + krow + (((cB + p4) ^ sR) * 16) + (p4 & 1) * 8;
    const unsigned boffB1 = lds + 65536 + gB * 8192 + krow + (((cB + p4) ^ sR) * 16) + ((p4 & 1) ^ 1) * 8;
    const unsigned boffS = lds + 65536 + gS * 8192 + krow + (((2 * wc + (p4 >> 1)) ^ sR) * 16) + (p4 & 1) * 8;

    f32x4 acc[8][5];
#pragma unroll
    for (int m = 0; m < 8; ++m)
#pragma unroll
        for (int n = 0; n < 5; ++n) acc[m][n] = (f32x4){0.f, 0.f, 0.f, 0.f};
    bf16x8 af[8], bfg[5];

    WAIT_V(9); BAR();
    STAMP(0, 1);
    if (wr == 1) BAR();

#define K1_SEG(b, ks, t) do { \
        { constexpr int _bo = (b) * 40960 + (ks) * 4096; \
          bfg[0] = cat8(trread(LDSP(boffA0) + _bo), trread(LDSP(boffA0) + _bo + 512)); bfg[1] = cat8(trread(LDSP(boffA1) + _bo), trread(LDSP(boffA1) + _bo + 512)); \
          bfg[2] = cat8(trread(LDSP(boffB0) + _bo), trread(LDSP(boffB0) + _bo + 512)); bfg[3] = cat8(trread(LDSP(boffB1) + _bo), trread(LDSP(boffB1) + _bo + 512)); \
          bfg[4] = cat8(trread(LDSP(boffS) + _bo), trread(LDSP(boffS) + _bo + 512)); \
          LAS const char* _ap = LDSP((ks) ? aoff1 : aoff0) + (b) * 32768; \
          _Pragma("unroll") for (int _m = 0; _m < 8; ++_m) af[_m] = *(LAS const bf16x8*)(_ap + _m * 2048); } \
        if ((ks) == 0) { const int _tt = ((t) + 1 < 16) ? (t) + 1 : 15; if (wr == 0) K1_DMA_A(1, _tt, (b) ^ 1); else K1_DMA_B(1, _tt, (b) ^ 1); } \
        else { const int _tt = ((t) + 2 < 16) ? (t) + 2 : 15; if (wr == 0) K1_DMA_B(0, _tt, (b)); else K1_DMA_A(0, _tt, (b)); } \
        WAIT_V(9); WAIT_L(0); SCHED(); BAR(); SCHED(); \
        __builtin_amdgcn_s_setprio(1); \
        _Pragma("unroll") for (int _m = 0; _m < 8; ++_m) _Pragma("unroll") for (int _n = 0; _n < 5; ++_n) \
            acc[_m][_n] = __builtin_amdgcn_mfma_f32_16x16x32_bf16(bfg[_n], af[_m], acc[_m][_n], 0, 0, 0); \
        __builtin_amdgcn_s_setprio(0); \
        SCHED(); BAR(); SCHED(); } while (0)

    for (int t = 0; t < 16; t += 2) {
        K1_SEG(0, 0, t); K1_SEG(0, 1, t); K1_SEG(1, 0, t + 1); K1_SEG(1, 1, t + 1);
    }
    if (wr == 0) BAR();
    STAMP(0, 2);

    const bool roleP = type1 || wc < 2;
    const int row0 = rb * 256 + wr * 128 + fr;
    bf16_t* Ap = (bf16_t*)(ws + WS_A);
    bf16_t* Qb = (bf16_t*)(ws + WS_Q); bf16_t* Kb = (bf16_t*)(ws + WS_K); bf16_t* Vb = (bf16_t*)(ws + WS_V);
    const bool odd = fq & 1;
    {
        const int gam = type1 ? t1 : 24 + 3 * t2;
        const int col = 64 * gam + 16 * wc + 4 * fq;
        const int part = col >> 10, head = (col & 1023) >> 5, d = col & 31;
        const float sc = part == 0 ? QSCALE : 1.0f;
#pragma unroll
        for (int m = 0; m < 8; ++m) {
            const int row = row0 + 16 * m;
            const f32x4 v = acc[m][4] * sc;
            u32x2 w; w.x = cvtpk(v[0], v[1]); w.y = cvtpk(v[2], v[3]);
            bf16_t* dst;
            if (part == 1) dst = Kb + (size_t)head * 65536 + (row >> 6) * 2048 + (d >> 3) * 512 + (row & 63) * 8 + (d & 7);
            else dst = (part == 0 ? Qb : Vb) + ((size_t)head * SEQ + row) * 32 + d;
            *(u32x2*)dst = w;
        }
    }
    if (roleP) {
        const int hcol = (type1 ? 128 * t1 : 3072 + 64 * t2) + 32 * wc + 8 * fq;
#pragma unroll
        for (int m = 0; m < 8; ++m) {
            const int row = row0 + 16 * m;
            float h0[4], h1[4];
#pragma unroll
            for (int j = 0; j < 4; ++j) { h0[j] = gelu_tanh(acc[m][0][j]) * acc[m][2][j]; h1[j] = gelu_tanh(acc[m][1][j]) * acc[m][3][j]; }
            const unsigned a0 = cvtpk(h0[0], h0[1]), a1 = cvtpk(h0[2], h0[3]), b0 = cvtpk(h1[0], h1[1]), b1 = cvtpk(h1[2], h1[3]);
            u32x4 w; w.x = odd ? b0 : a0; w.y = odd ? b1 : a1; w.z = odd ? a0 : b0; w.w = odd ? a1 : b1;
            *(u32x4*)(Ap + (size_t)row * KTOT + hcol) = w;
        }
    } else {
#pragma unroll
        for (int pr = 0; pr < 2; ++pr) {
            const int gam = 24 + 3 * t2 + (wc - 1);
            const int col = 64 * gam + 32 * pr + 8 * fq;
            const int part = col >> 10, head = (col & 1023) >> 5, d = col & 31;
            const float sc = part == 0 ? QSCALE : 1.0f;
#pragma unroll
            for (int m = 0; m < 8; ++m) {
                const int row = row0 + 16 * m;
                const f32x4 v0 = acc[m][2 * pr] * sc, v1 = acc[m][2 * pr + 1] * sc;
                const unsigned a0 = cvtpk(v0[0], v0[1]), a1 = cvtpk(v0[2], v0[3]), b0 = cvtpk(v1[0], v1[1]), b1 = cvtpk(v1[2], v1[3]);
                u32x4 w; w.x = odd ? b0 : a0; w.y = odd ? b1 : a1; w.z = odd ? a0 : b0; w.w = odd ? a1 : b1;
                bf16_t* dst;
                if (part == 1) dst = Kb + (size_t)head * 65536 + (row >> 6) * 2048 + (d >> 3) * 512 + (row & 63) * 8;
                else dst = (part == 0 ? Qb : Vb) + ((size_t)head * SEQ + row) * 32 + d;
                *(u32x4*)dst = w;
            }
        }
    }
    STAMP(0, 3);
    WAIT_V(0);
    STAMP(0, 4);
#undef K1_SEG
#undef K1_DMA_A
#undef K1_DMA_B
}

constexpr int K3_STG = 49152;
constexpr int K23_SCR = 3 * K3_STG;
constexpr int K23_LDS = K23_SCR + 1024;
#define K2_RING(j) (K3_STG + (((j) + 1) % 3) * 32768)
__device__ __forceinline__ int crow(int r, int hi) { return (r & 3) + 8 * (r >> 2) + 4 * hi; }
#define MFMA32(a, b, c) __builtin_amdgcn_mfma_f32_32x32x16_bf16(a, b, c, 0, 0, 0)
#define MFMA16(a, b, c) __builtin_amdgcn_mfma_f32_16x16x32_bf16(a, b, c, 0, 0, 0)
#define MX3(a, b, c) __builtin_fmaxf(__builtin_fmaxf((a), (b)), (c))
#define PIN(x) asm volatile("" : "+v"(x))
#define EX2(v) __builtin_amdgcn_exp2f(v)
__device__ __forceinline__ void glds4_sc1(const char* sbase, unsigned voff, unsigned ldsaddr) {
    asm volatile("s_mov_b32 m0, %2\n\ts_nop 0\n\tglobal_load_lds_dword %0, %1 sc1" :: "v"(voff), "s"(sbase), "s"(ldsaddr) : "memory"); }
__device__ __forceinline__ void glds4(const char* sbase, unsigned voff, unsigned ldsaddr) {
    asm volatile("s_mov_b32 m0, %2\n\ts_nop 0\n\tglobal_load_lds_dword %0, %1" :: "v"(voff), "s"(sbase), "s"(ldsaddr) : "memory"); }

__global__ __launch_bounds__(768, 3) void k23(const bf16_t* __restrict__ wo, const bf16_t* __restrict__ wd, unsigned char* __restrict__ ws, bf16_t* __restrict__ out) {
    extern __shared__ __attribute__((aligned(16))) char smem[];
    const unsigned lds = (unsigned)(uintptr_t)(LAS char*)smem;
    const int tid = threadIdx.x, wid = __builtin_amdgcn_readfirstlane(tid >> 6), lane = tid & 63;
    const int bid = blockIdx.x, xcd = bid & 7, idx = bid >> 3;
    unsigned* flags = (unsigned*)(ws + WS_FLAG);
    const int kq = xcd >> 1, mb = (xcd & 1) * 4 + (idx >> 3), nb = idx & 7;
    const int head = xcd * 4 + (idx & 3), qb = idx >> 2;
    LAS unsigned* fl = (LAS unsigned*)(uintptr_t)(lds + K23_SCR);
    CODE_LABEL("gm_k23_beg");

    if (wid >= 8) {
        const int L = wid - 8;
        const int lr = lane >> 3, ls = lane & 7;
        const char* Kg = (const char*)(ws + WS_K) + (size_t)head * 131072;
        const char* Vg = (const char*)(ws + WS_V) + (size_t)head * 131072;
        const unsigned voff = (unsigned)(L * 4096 + lane * 16);
#define K2_DMA(j) do { const char* _k = Kg + (size_t)(j) * 16384; const char* _v = Vg + (size_t)(j) * 16384; const unsigned _d = lds + K2_RING(j) + L * 4096; \
        _Pragma("unroll") for (int _i = 0; _i < 4; ++_i) glds16(_k + _i * 1024, voff, _d + _i * 1024); \
        _Pragma("unroll") for (int _i = 0; _i < 4; ++_i) glds16(_v + _i * 1024, voff, _d + 16384 + _i * 1024); } while (0)
        const unsigned voffA0 = (unsigned)(lr * (KTOT * 2) + ((ls ^ ((lane >> 4) & 7)) * 16)), voffA1 = (unsigned)(lr * (KTOT * 2) + ((ls ^ ((4 + (lane >> 4)) & 7)) * 16));
        const unsigned voffB0 = (unsigned)(lr * (DM * 2) + ((ls ^ (3 * ((lane >> 4) & 1))) * 16)), voffB1 = (unsigned)(lr * (DM * 2) + ((ls ^ (3 * ((lane >> 4) & 1) + 4)) * 16));
        const char* Ag = (const char*)(ws + WS_A) + (size_t)(mb * 256 + L * 32) * KTOT * 2;
#define K3_DMA_A(tt, h) do { const int _t = (tt) < 18 ? (tt) : 17; const unsigned _d = lds + ((tt) % 3) * K3_STG + (h) * 16384 + L * 4096; \
        const char* _s = Ag + (_t < 14 ? (14 * kq + _t) * 128 : FF * 2 + (4 * kq + _t - 14) * 128) + (size_t)((h) * 128) * KTOT * 2; \
        glds16(_s, voffA0, _d); glds16(_s + (size_t)8 * KTOT * 2, voffA1, _d + 1024); glds16(_s + (size_t)16 * KTOT * 2, voffA0, _d + 2048); glds16(_s + (size_t)24 * KTOT * 2, voffA1, _d + 3072); } while (0)
#define K3_DMA_B(tt, g) do { const int _t = (tt) < 18 ? (tt) : 17; const unsigned _d = lds + ((tt) % 3) * K3_STG + 32768 + (g) * 8192 + L * 2048; \
        const char* _w = (_t < 14 ? (const char*)wd + (size_t)(14 * kq + _t) * 64 * DM * 2 : (const char*)wo + (size_t)(4 * kq + _t - 14) * 64 * DM * 2) + nb * 256 + (g) * 128 + (size_t)(L * 16) * DM * 2; \
        glds16(_w, voffB0, _d); glds16(_w + (size_t)8 * DM * 2, voffB1, _d + 1024); } while (0)
        K2_DMA(0); K2_DMA(1);
        {
            const char* cb; int cl;
            CODE_RANGE(cb, cl, "gm_k23_ref", "gm_k23_beg", "gm_k23_end");
            const int off = ((L & 1) * 64 + lane) * 128;
            if (L >= 2) CODE_RANGE(cb, cl, "gm_k23_ref2", "gm_k23_ref2", "gm_k23_end2");
            if (cl > 0) glds4(cb, (unsigned)(off < cl - 4 ? off : 0), lds + K23_SCR + 256);
        }
        WAIT_V(0); BAR();
#pragma unroll
        for (int j = 0; j < 8; ++j) {
            if (j == 1 || j == 2) { WAIT_V(0); BAR(); } else if (j >= 3 && j <= 5) { WAIT_V(1); BAR(); } else if (j == 6) { WAIT_V(13); BAR(); } else if (j == 7) { BAR(); }
            if (j + 2 < 8) K2_DMA(j + 2);
            if (j >= 2 && j <= 5) {
                const int ins = (j - 2) * 4 + L;
                if (ins < 9) {
                    const int row0 = (idx * 9 + ins) * 4;
                    const char* base = row0 < 896 ? (const char*)wd + (size_t)(896 * kq + row0) * 2048 : (const char*)wo + (size_t)(256 * kq + row0 - 896) * 2048;
                    glds4(base, (unsigned)(lane * 128), lds + K23_SCR + 256);
                } else {
                    const int e = ins < 14 ? ins - 9 : ins - 11;
                    const int x = e * 32 + idx;
                    const char* base = (const char*)(ws + WS_A) + (size_t)((xcd & 1) * 1024 + (x & 15) * 64) * (KTOT * 2) + (14 * kq + (x >> 4)) * 128;
                    glds4(base, (unsigned)(lane * (KTOT * 2)), lds + K23_SCR + 256);
                }
            }
            if (j == 5) { K3_DMA_A(0, 0); K3_DMA_B(0, 0); K3_DMA_B(0, 1); K3_DMA_A(0, 1); }
            if (j == 6) { K3_DMA_A(1, 0); K3_DMA_A(1, 1); }
            if (j == 7) { K3_DMA_B(1, 0); K3_DMA_B(1, 1); }
        }
        BAR(); BAR();
        const unsigned redo = __builtin_amdgcn_readfirstlane(fl[0]);
        if (redo) {
            WAIT_V(0); BAR();
            K2_DMA(0); K2_DMA(1);
            for (int j = 0; j < 8; ++j) {
                if (j < 7) WAIT_V(8); else WAIT_V(0);
                BAR();
                if (j + 2 < 8) K2_DMA(j + 2);
            }
            BAR();
            K3_DMA_A(1, 0); K3_DMA_B(1, 0); K3_DMA_B(1, 1); K3_DMA_A(1, 1);
        }
        WAIT_V(0); BAR();
#define K3_DMA_T(tt) do { K3_DMA_A(tt, 0); K3_DMA_B(tt, 0); K3_DMA_B(tt, 1); K3_DMA_A(tt, 1); } while (0)
        K3_DMA_T(2);
        int ost = 0;
        for (int t = 0; t < 18; ++t) {
            if (L == 0 && t >= 4 && t <= 11) {
                if (t == 8 || t == 10 || t == 11) {
                    WAIT_V(12);
                    if (ost == 0) {
                        if ((unsigned)__builtin_amdgcn_readfirstlane(fl[128]) >= 8u) ost = 2;
                        else if (t == 11) {
                            unsigned spins = 0;
                            while (__builtin_amdgcn_readfirstlane(__hip_atomic_load(flags + (256 + mb * 4 + kq) * 64, RLX_AGENT)) < 8u) { __builtin_amdgcn_s_sleep(1); if (++spins > (1u << 22)) break; }
                            ost = 2;
                        }
                        if (ost == 2 && t == 11) { __builtin_amdgcn_fence(__ATOMIC_ACQUIRE, "agent"); WAIT_V(0); ost = 1; }
                    } else if (t == 11 && ost == 3) WAIT_V(0);
                } else WAIT_V(13);
            } else if (t < 16) WAIT_V(12);
            else WAIT_V(0);
            BAR();
            if (t + 3 < 18) K3_DMA_T(t + 3);
            if (L == 0) {
                if (t == 3) { if (lane == 0) __hip_atomic_fetch_add(flags + (256 + qb * 4 + (xcd >> 1)) * 64, 1u, RLX_AGENT); }
                else if (t == 5 || (t == 8 && ost == 0)) glds4_sc1((const char*)(flags + (256 + mb * 4 + kq) * 64), 0u, lds + K23_SCR + 512);
                else if (ost == 2) { asm volatile("buffer_inv sc1" ::: "memory"); ost = (t == 10) ? 3 : 1; }
            }
        }
#undef K3_DMA_T
        CODE_LABEL("gm_k23_end2");
        return;
#undef K2_DMA
#undef K3_DMA_A
#undef K3_DMA_B
    }

    STAMP(1, 0);
    float omg[4];
    { const float* gv = (const float*)(ws + WS_G) + mb * 256 + (wid >> 2) * 128 + ((wid & 3) >> 1) * 64 + (lane & 15);
#pragma unroll
      for (int m = 0; m < 4; ++m) omg[m] = 1.0f - gv[16 * m]; }

    {
        const int r32 = lane & 31, hi = lane >> 5;
        const bf16_t* Qg = (const bf16_t*)(ws + WS_Q) + ((size_t)head * SEQ + qb * 256 + wid * 32) * 32;
        bf16x8 qf0 = *(const bf16x8*)(Qg + r32 * 32 + hi * 8);
        bf16x8 qf1 = *(const bf16x8*)(Qg + r32 * 32 + 16 + hi * 8);
        float gval = ((const float*)(ws + WS_G))[qb * 256 + wid * 32 + r32];
        bf16x8 ones;
        { const short o1 = (((lane >> 4) & 1) == (((lane & 15) >> 2) & 1)) ? (short)0x3F80 : (short)0;
#pragma unroll
          for (int j = 0; j < 8; ++j) ones[j] = o1; }
        const unsigned koff = lds + hi * 1024 + r32 * 16;
        const unsigned voffr = lds + 16384 + ((lane >> 4) & 1) * 32 + (lane & 3) * 8 + (4 * hi + ((lane & 15) >> 2)) * 64;
        const unsigned koff0 = koff + K2_RING(0);
        f32x16 o = {}; f32x4 lsum = {0.f, 0.f, 0.f, 0.f};
        asm volatile("" : "+v"(omg[0]), "+v"(omg[1]), "+v"(omg[2]), "+v"(omg[3]), "+v"(gval), "+v"(qf0), "+v"(qf1));
        BAR();
        STAMP(1, 1);
        {
            f32x16 SA, SB, negm;
            u32x4 WA0, WA1, WB0 = {0u, 0u, 0u, 0u}, WB1 = {0u, 0u, 0u, 0u};
            bf16x8 kf0, kf1, vf0 = {}, vf1 = {};
            kf0 = *(LAS const bf16x8*)(LDSP(koff0)); kf1 = *(LAS const bf16x8*)(LDSP(koff0) + 2048);
            { f32x16 z = {}; SA = MFMA32(kf0, qf0, z); SA = MFMA32(kf1, qf1, SA); }
            kf0 = *(LAS const bf16x8*)(LDSP(koff0) + 512); kf1 = *(LAS const bf16x8*)(LDSP(koff0) + 2048 + 512);
            {
                float a = MX3(SA[0], SA[1], SA[2]), b = MX3(SA[3], SA[4], SA[5]);
                a = MX3(a, SA[6], SA[7]); b = MX3(b, SA[8], SA[9]); a = MX3(a, SA[10], SA[11]); b = MX3(b, SA[12], SA[13]); a = MX3(a, SA[14], SA[15]);
                float rm = __builtin_fmaxf(a, b);
                auto rr = __builtin_amdgcn_permlane32_swap(__float_as_uint(rm), __float_as_uint(rm), false, false); rm = __builtin_fmaxf(__uint_as_float(rr[0]), __uint_as_float(rr[1]));
#pragma unroll
                for (int r = 0; r < 16; ++r) { SA[r] -= rm; negm[r] = -rm; }
                PIN(negm);
            }
#define K2_STEP(C, N, WP0, WP1, WC0, WC1, KADDR, VADDR) do { \
            N = MFMA32(kf0, qf0, negm); \
            C[0] = EX2(C[0]); C[1] = EX2(C[1]); C[2] = EX2(C[2]); C[3] = EX2(C[3]); PIN(C); SCHED(); \
            kf0 = *(LAS const bf16x8*)(KADDR); SCHED(); \
            o = MFMA32(vf0, __builtin_bit_cast(bf16x8, WP0), o); \
            C[4] = EX2(C[4]); C[5] = EX2(C[5]); C[6] = EX2(C[6]); C[7] = EX2(C[7]); PIN(C); SCHED(); \
            vf0 = cat8(trread(VADDR), trread((VADDR) + 512)); SCHED(); \
            lsum = MFMA16(ones, __builtin_bit_cast(bf16x8, WP0), lsum); \
            WC0[0] = cvtpk(C[0], C[1]); WC0[1] = cvtpk(C[2], C[3]); WC0[2] = cvtpk(C[4], C[5]); WC0[3] = cvtpk(C[6], C[7]); PIN(WC0); SCHED(); \
            N = MFMA32(kf1, qf1, N); \
            C[8] = EX2(C[8]); C[9] = EX2(C[9]); C[10] = EX2(C[10]); C[11] = EX2(C[11]); PIN(C); SCHED(); \
            kf1 = *(LAS const bf16x8*)((KADDR) + 2048); SCHED(); \
            o = MFMA32(vf1, __builtin_bit_cast(bf16x8, WP1), o); \
            C[12] = EX2(C[12]); C[13] = EX2(C[13]); C[14] = EX2(C[14]); C[15] = EX2(C[15]); PIN(C); SCHED(); \
            vf1 = cat8(trread((VADDR) + 1024), trread((VADDR) + 1536)); SCHED(); \
            lsum = MFMA16(ones, __builtin_bit_cast(bf16x8, WP1), lsum); \
            WC1[0] = cvtpk(C[8], C[9]); WC1[1] = cvtpk(C[10], C[11]); WC1[2] = cvtpk(C[12], C[13]); WC1[3] = cvtpk(C[14], C[15]); PIN(WC1); SCHED(); \
            } while (0)
            for (int j = 0; j < 8; ++j) {
                if (j > 0) { WAIT_L(0); BAR(); }
                LAS const char* kc = LDSP(koff + K2_RING(j));
                LAS const char* kn = LDSP(koff + K2_RING(j + 1));
                LAS const char* vc = LDSP(voffr + K2_RING(j));
                K2_STEP(SA, SB, WB0, WB1, WA0, WA1, kc + 1 * 4096, vc + 0 * 2048);
                K2_STEP(SB, SA, WA0, WA1, WB0, WB1, kc + 1 * 4096 + 512, vc + 1 * 2048);
                K2_STEP(SA, SB, WB0, WB1, WA0, WA1, kc + 2 * 4096, vc + 2 * 2048);
                K2_STEP(SB, SA, WA0, WA1, WB0, WB1, kc + 2 * 4096 + 512, vc + 3 * 2048);
                K2_STEP(SA, SB, WB0, WB1, WA0, WA1, kc + 3 * 4096, vc + 4 * 2048);
                K2_STEP(SB, SA, WA0, WA1, WB0, WB1, kc + 3 * 4096 + 512, vc + 5 * 2048);
                K2_STEP(SA, SB, WB0, WB1, WA0, WA1, kn, vc + 6 * 2048);
                K2_STEP(SB, SA, WA0, WA1, WB0, WB1, kn + 512, vc + 7 * 2048);
            }
            o = MFMA32(vf0, __builtin_bit_cast(bf16x8, WB0), o); lsum = MFMA16(ones, __builtin_bit_cast(bf16x8, WB0), lsum);
            o = MFMA32(vf1, __builtin_bit_cast(bf16x8, WB1), o); lsum = MFMA16(ones, __builtin_bit_cast(bf16x8, WB1), lsum);
#undef K2_STEP
        }
        STAMP(1, 2);
        {
            const bool bad = !(lsum[0] <= 1.0e9f);
            if (tid == 0) fl[0] = 0u;
            WAIT_L(0); BAR();
            if (__any(bad) && lane == 0) fl[0] = 1u;
            WAIT_L(0); BAR();
            const unsigned redo = __builtin_amdgcn_readfirstlane(fl[0]);
            if (redo) {
                WAIT_L(0); BAR();
                o = (f32x16){}; lsum = (f32x4){0.f, 0.f, 0.f, 0.f};
                float mhat = 0.f; f32x16 negm = {};
                bool first = true;
                for (int j = 0; j < 8; ++j) {
                    WAIT_L(0); BAR();
                    LAS const char* kb = LDSP(koff + K2_RING(j));
                    LAS const char* vb = LDSP(voffr + K2_RING(j));
#pragma unroll 1
                    for (int tt = 0; tt < 4; ++tt) {
                        LAS const char* kp = kb + tt * 4096;
                        LAS const char* vp = vb + tt * 4096;
                        const bf16x8 k00 = *(LAS const bf16x8*)(kp), k01 = *(LAS const bf16x8*)(kp + 2048), k10 = *(LAS const bf16x8*)(kp + 512), k11 = *(LAS const bf16x8*)(kp + 2048 + 512);
                        f32x16 p0 = MFMA32(k00, qf0, negm); p0 = MFMA32(k01, qf1, p0);
                        f32x16 p1 = MFMA32(k10, qf0, negm); p1 = MFMA32(k11, qf1, p1);
                        float a = MX3(p0[0], p0[1], p1[0]), b = MX3(p0[2], p0[3], p1[1]); a = MX3(a, p1[2], p1[3]);
#pragma unroll
                        for (int r = 4; r < 16; r += 4) { a = MX3(a, p0[r], p0[r + 1]); b = MX3(b, p0[r + 2], p0[r + 3]); a = MX3(a, p1[r], p1[r + 1]); b = MX3(b, p1[r + 2], p1[r + 3]); }
                        float rm = __builtin_fmaxf(a, b);
                        { auto rr = __builtin_amdgcn_permlane32_swap(__float_as_uint(rm), __float_as_uint(rm), false, false); rm = __builtin_fmaxf(__uint_as_float(rr[0]), __uint_as_float(rr[1])); }
                        {
                            const float dl = first ? rm : __builtin_fmaxf(rm, 0.f);
                            mhat += dl;
#pragma unroll
                            for (int r = 0; r < 16; ++r) { p0[r] -= dl; p1[r] -= dl; negm[r] = -mhat; }
                            if (!first) { const float f = EX2(-dl); o = o * f; lsum = lsum * f; }
                            first = false;
                        }
#pragma unroll
                        for (int r = 0; r < 16; ++r) { p0[r] = EX2(p0[r]); p1[r] = EX2(p1[r]); }
                        const u32x4 w0 = {cvtpk(p0[0], p0[1]), cvtpk(p0[2], p0[3]), cvtpk(p0[4], p0[5]), cvtpk(p0[6], p0[7])};
                        const u32x4 w1 = {cvtpk(p0[8], p0[9]), cvtpk(p0[10], p0[11]), cvtpk(p0[12], p0[13]), cvtpk(p0[14], p0[15])};
                        const u32x4 w2 = {cvtpk(p1[0], p1[1]), cvtpk(p1[2], p1[3]), cvtpk(p1[4], p1[5]), cvtpk(p1[6], p1[7])};
                        const u32x4 w3 = {cvtpk(p1[8], p1[9]), cvtpk(p1[10], p1[11]), cvtpk(p1[12], p1[13]), cvtpk(p1[14], p1[15])};
                        const bf16x8 v0 = cat8(trread(vp), trread(vp + 512)), v1 = cat8(trread(vp + 1024), trread(vp + 1536));
                        const bf16x8 v2 = cat8(trread(vp + 2048), trread(vp + 2560)), v3 = cat8(trread(vp + 3072), trread(vp + 3584));
                        const bf16x8 f0 = __builtin_bit_cast(bf16x8, w0), f1 = __builtin_bit_cast(bf16x8, w1), f2 = __builtin_bit_cast(bf16x8, w2), f3 = __builtin_bit_cast(bf16x8, w3);
                        o = MFMA32(v0, f0, o); lsum = MFMA16(ones, f0, lsum);
                        o = MFMA32(v1, f1, o); lsum = MFMA16(ones, f1, lsum);
                        o = MFMA32(v2, f2, o); lsum = MFMA16(ones, f2, lsum);
                        o = MFMA32(v3, f3, o); lsum = MFMA16(ones, f3, lsum);
                    }
                }
                WAIT_L(0); BAR();
            }
        }
        {
            const float sc = gval * __builtin_amdgcn_rcpf(lsum[0]);
            unsigned w[8];
#pragma unroll
            for (int g = 0; g < 4; ++g) { w[2 * g] = cvtpk(o[4 * g] * sc, o[4 * g + 1] * sc); w[2 * g + 1] = cvtpk(o[4 * g + 2] * sc, o[4 * g + 3] * sc); }
            const __amdgpu_buffer_rsrc_t rs = __builtin_amdgcn_make_buffer_rsrc((void*)(ws + WS_A), (short)0, (int)(SEQ * KTOT * 2), 0x00020000);
            const unsigned ob = (unsigned)(((qb * 256 + wid * 32 + r32) * KTOT + FF + head * 32 + 8 * hi) * 2);
#pragma unroll
            for (int pr = 0; pr < 2; ++pr) {
                auto r0 = __builtin_amdgcn_permlane32_swap(w[4 * pr], w[4 * pr + 2], false, false);
                auto r1 = __builtin_amdgcn_permlane32_swap(w[4 * pr + 1], w[4 * pr + 3], false, false);
                const u32x4 v = {r0[0], r1[0], r0[1], r1[1]};
                __builtin_amdgcn_raw_buffer_store_b128(v, rs, ob + pr * 32, 0, 16);
            }
            STAMP(1, 3);
            WAIT_L(0); BAR();
            STAMP(1, 4);
        }
    }

    {
        const int G = wid >> 2, j4 = wid & 3, wm = j4 >> 1, wn = j4 & 1, fr = lane & 15, fq = lane >> 4;
        const int arow = G * 128 + wm * 64 + fr;
        const unsigned aoff0 = lds + arow * 128 + ((fq ^ (fr >> 1)) * 16);
        const unsigned aoff1 = lds + arow * 128 + (((fq ^ (fr >> 1)) ^ 4) * 16);
        const int q4 = (lane >> 2) & 3, p4 = lane & 3;
        const int sR = 3 * ((q4 >> 1) & 1) + 4 * (fq & 1);
        const int krow = (8 * fq + q4) * 128;
        const unsigned boff00 = lds + 32768 + wn * 8192 + krow + ((p4 ^ sR) * 16) + (p4 & 1) * 8;
        const unsigned boff01 = lds + 32768 + wn * 8192 + krow + ((p4 ^ sR) * 16) + ((p4 & 1) ^ 1) * 8;
        const unsigned boff10 = lds + 32768 + wn * 8192 + krow + (((4 + p4) ^ sR) * 16) + (p4 & 1) * 8;
        const unsigned boff11 = lds + 32768 + wn * 8192 + krow + (((4 + p4) ^ sR) * 16) + ((p4 & 1) ^ 1) * 8;
        f32x4 acc[4][4];
#pragma unroll
        for (int m = 0; m < 4; ++m)
#pragma unroll
            for (int n = 0; n < 4; ++n) acc[m][n] = (f32x4){0.f, 0.f, 0.f, 0.f};
        unsigned fts = 0;
        STAMP(2, 0);
        STAMP(2, 1);
#define K3_READ(F_A, F_B, so_, ks_) do { \
            F_B[0] = cat8(trread(LDSP(boff00 + (so_)) + (ks_) * 4096), trread(LDSP(boff00 + (so_)) + (ks_) * 4096 + 512)); \
            F_A[0] = *(LAS const bf16x8*)(LDSP(((ks_) ? aoff1 : aoff0) + (so_))); \
            F_B[1] = cat8(trread(LDSP(boff01 + (so_)) + (ks_) * 4096), trread(LDSP(boff01 + (so_)) + (ks_) * 4096 + 512)); \
            F_A[1] = *(LAS const bf16x8*)(LDSP(((ks_) ? aoff1 : aoff0) + (so_)) + 2048); \
            F_B[2] = cat8(trread(LDSP(boff10 + (so_)) + (ks_) * 4096), trread(LDSP(boff10 + (so_)) + (ks_) * 4096 + 512)); \
            F_A[2] = *(LAS const bf16x8*)(LDSP(((ks_) ? aoff1 : aoff0) + (so_)) + 4096); \
            F_B[3] = cat8(trread(LDSP(boff11 + (so_)) + (ks_) * 4096), trread(LDSP(boff11 + (so_)) + (ks_) * 4096 + 512)); \
            F_A[3] = *(LAS const bf16x8*)(LDSP(((ks_) ? aoff1 : aoff0) + (so_)) + 6144); } while (0)
#define K3_RB(dst, boff, so_, ks_) dst = cat8(trread(LDSP((boff) + (so_)) + (ks_) * 4096), trread(LDSP((boff) + (so_)) + (ks_) * 4096 + 512))
#define K3_RA(dst, so_, ks_, m_) dst = *(LAS const bf16x8*)(LDSP(((ks_) ? aoff1 : aoff0) + (so_)) + (m_) * 2048)
#define K3_M(CA, CB, m_, n_) acc[m_][n_] = MFMA16(CB[n_], CA[m_], acc[m_][n_])
#define K3_STEP(CA, CB, NA, NB, so_, ks_) do { \
            K3_M(CA, CB, 0, 0); K3_RA(NA[0], so_, ks_, 0); SCHED(); \
            K3_M(CA, CB, 0, 1); K3_RB(NB[0], boff00, so_, ks_); SCHED(); \
            K3_M(CA, CB, 0, 2); SCHED(); \
            K3_M(CA, CB, 0, 3); K3_RB(NB[1], boff01, so_, ks_); SCHED(); \
            K3_M(CA, CB, 1, 0); SCHED(); \
            K3_M(CA, CB, 1, 1); K3_RB(NB[2], boff10, so_, ks_); SCHED(); \
            K3_M(CA, CB, 1, 2); SCHED(); \
            K3_M(CA, CB, 1, 3); K3_RB(NB[3], boff11, so_, ks_); SCHED(); \
            K3_M(CA, CB, 2, 0); SCHED(); \
            K3_M(CA, CB, 2, 1); K3_RA(NA[1], so_, ks_, 1); SCHED(); \
            K3_M(CA, CB, 2, 2); K3_RA(NA[2], so_, ks_, 2); SCHED(); \
            K3_M(CA, CB, 2, 3); K3_RA(NA[3], so_, ks_, 3); SCHED(); \
            K3_M(CA, CB, 3, 0); K3_M(CA, CB, 3, 1); K3_M(CA, CB, 3, 2); K3_M(CA, CB, 3, 3); SCHED(); } while (0)
        bf16x8 a0[4], b0[4], a1[4], b1[4];
        K3_READ(a0, b0, 0u, 0);
        for (int t = 0; t < 18; ++t) {
            const unsigned so = (t % 3) * K3_STG, sn = ((t + 1) % 3) * K3_STG;
            if (t == 14) {
#pragma unroll
                for (int m = 0; m < 4; ++m)
#pragma unroll
                    for (int n = 0; n < 4; ++n) acc[m][n] = acc[m][n] * omg[m];
            }
            SCHED();
            K3_STEP(a0, b0, a1, b1, so, 1);
            if (t == 3) WAIT_V(0);
            WAIT_L(0); SCHED(); BAR(); SCHED();
            K3_STEP(a1, b1, a0, b0, sn, 0);
        }
#undef K3_STEP
#undef K3_M
#undef K3_RA
#undef K3_RB
#undef K3_READ
        STAMP(2, 2);
        {
            const __amdgpu_buffer_rsrc_t rs = __builtin_amdgcn_make_buffer_rsrc((void*)(ws + WS_P), (short)0, (int)(256 * 65536), 0x00020000);
            const unsigned so = (unsigned)(bid * 65536 + wid * 2048 + lane * 16);
            f32x4 fin[4];
#define K3_SEND(q) do { \
                const u32x4 w0 = {cvtpk(acc[q][0][0], acc[q][0][1]), cvtpk(acc[q][0][2], acc[q][0][3]), cvtpk(acc[q][1][0], acc[q][1][1]), cvtpk(acc[q][1][2], acc[q][1][3])}; \
                const u32x4 w1 = {cvtpk(acc[q][2][0], acc[q][2][1]), cvtpk(acc[q][2][2], acc[q][2][3]), cvtpk(acc[q][3][0], acc[q][3][1]), cvtpk(acc[q][3][2], acc[q][3][3])}; \
                __builtin_amdgcn_raw_buffer_store_b128(w0, rs, so + (q) * 16384, 0, 16); __builtin_amdgcn_raw_buffer_store_b128(w1, rs, so + (q) * 16384 + 1024, 0, 16); } while (0)
#define K3_KEEP(q) do { _Pragma("unroll") for (int n = 0; n < 4; ++n) fin[n] = acc[q][n]; } while (0)
            if (kq == 0) { K3_SEND(1); K3_SEND(2); K3_SEND(3); K3_KEEP(0); }
            else if (kq == 1) { K3_SEND(0); K3_SEND(2); K3_SEND(3); K3_KEEP(1); }
            else if (kq == 2) { K3_SEND(0); K3_SEND(1); K3_SEND(3); K3_KEEP(2); }
            else { K3_SEND(0); K3_SEND(1); K3_SEND(2); K3_KEEP(3); }
#undef K3_SEND
#undef K3_KEEP
            STAMP(2, 3);
            WAIT_V(0);
            STAMP(2, 4);
            unsigned* xcnt = (unsigned*)(ws + WS_XC);
            if (lane < 3) { const int q = (kq + 1 + lane) & 3; __hip_atomic_fetch_add(xcnt + ((idx * 8 + 2 * q + (xcd & 1)) * 8 + wid) * 32, 1u, RLX_AGENT); }
            {
                unsigned spins = 0;
                while (__builtin_amdgcn_readfirstlane(__hip_atomic_load(xcnt + (bid * 8 + wid) * 32, RLX_AGENT)) < 3u) { __builtin_amdgcn_s_sleep(1); if (++spins > (1u << 22)) break; }
            }
            asm volatile("" ::: "memory");
            STAMP(2, 5);
            u32x4 rv[3][2];
#pragma unroll
            for (int i = 0; i < 3; ++i) {
                const int q = (kq + 1 + i) & 3;
                const unsigned po = (unsigned)((idx * 8 + 2 * q + (xcd & 1)) * 65536 + kq * 16384 + wid * 2048 + lane * 16);
                rv[i][0] = __builtin_amdgcn_raw_buffer_load_b128(rs, po, 0, 16); rv[i][1] = __builtin_amdgcn_raw_buffer_load_b128(rs, po + 1024, 0, 16);
            }
#pragma unroll
            for (int i = 0; i < 3; ++i)
#pragma unroll
                for (int n = 0; n < 4; ++n) {
                    const unsigned u0 = rv[i][n >> 1][(n & 1) * 2], u1 = rv[i][n >> 1][(n & 1) * 2 + 1];
                    fin[n][0] += __uint_as_float(u0 << 16); fin[n][1] += __uint_as_float(u0 & 0xffff0000u);
                    fin[n][2] += __uint_as_float(u1 << 16); fin[n][3] += __uint_as_float(u1 & 0xffff0000u);
                }
            const int row = mb * 256 + G * 128 + wm * 64 + kq * 16 + fr;
            const bool odd = fq & 1;
#pragma unroll
            for (int pr = 0; pr < 2; ++pr) {
                const f32x4 v0 = fin[2 * pr], v1 = fin[2 * pr + 1];
                const unsigned a0 = cvtpk(v0[0], v0[1]), a1 = cvtpk(v0[2], v0[3]), b0 = cvtpk(v1[0], v1[1]), b1 = cvtpk(v1[2], v1[3]);
                u32x4 w; w.x = odd ? b0 : a0; w.y = odd ? b1 : a1; w.z = odd ? a0 : b0; w.w = odd ? a1 : b1;
                *(u32x4*)(out + (size_t)row * DM + nb * 128 + wn * 64 + pr * 32 + 8 * fq) = w;
            }
        }
        STAMP(2, 6);
        WAIT_V(0);
        STAMP(2, 7);
    }
    CODE_LABEL("gm_k23_end");
}

extern "C" void kernel_launch(void* const* d_in, const int* in_sizes, int n_in, void* d_out, int out_size, void* d_ws, size_t ws_size, hipStream_t stream) {
    const bf16_t* x = (const bf16_t*)d_in[0];
    const bf16_t* wqkv = (const bf16_t*)d_in[1];
    const bf16_t* wo = (const bf16_t*)d_in[2];
    const bf16_t* wg = (const bf16_t*)d_in[4];
    const bf16_t* wu = (const bf16_t*)d_in[5];
    const bf16_t* wd = (const bf16_t*)d_in[6];
    const bf16_t* gw = (const bf16_t*)d_in[7];
    unsigned char* ws = (unsigned char*)d_ws;
    (void)hipFuncSetAttribute((const void*)k1_gemm, hipFuncAttributeMaxDynamicSharedMemorySize, K1_LDS);
    (void)hipFuncSetAttribute((const void*)k23, hipFuncAttributeMaxDynamicSharedMemorySize, K23_LDS);
    k1_gemm<<<256, 512, K1_LDS, stream>>>(x, wqkv, wg, wu, gw, ws);
    k23<<<256, 768, K23_LDS, stream>>>(wo, wd, ws, (bf16_t*)d_out);
}
```

```cpp
#include <hip/hip_runtime.h>
#include <hip/hip_bf16.h>
#include <stdint.h>

#define LAS __attribute__((address_space(3)))
typedef unsigned short bf16_t;
typedef short bf16x8 __attribute__((ext_vector_type(8)));
typedef short s16x4 __attribute__((ext_vector_type(4)));
typedef short v4i16_t __attribute__((ext_vector_type(4)));
typedef float f32x2 __attribute__((ext_vector_type(2)));
typedef float f32x4 __attribute__((ext_vector_type(4)));
typedef float f32x16 __attribute__((ext_vector_type(16)));
typedef unsigned u32x2 __attribute__((ext_vector_type(2)));
typedef unsigned u32x4 __attribute__((ext_vector_type(4)));
typedef __bf16 bf16x2_t __attribute__((ext_vector_type(2)));

constexpr int SEQ = 2048, DM = 1024, NQKV = 3072, FF = 3584, NH = 32, DH = 32;
constexpr int KTOT = FF + DM;
constexpr size_t WS_FLAG = 0;
constexpr size_t WS_G = 131072;
constexpr size_t WS_XC = 262144;
constexpr size_t WS_Q = 1u << 20;
constexpr size_t WS_K = WS_Q + (4u << 20);
constexpr size_t WS_V = WS_K + (4u << 20);
constexpr size_t WS_A = WS_V + (4u << 20);
constexpr size_t WS_P = WS_A + (20u << 20);

#define WAIT_V(n) asm volatile("s_waitcnt vmcnt(" #n ")" ::: "memory")
#define WAIT_L(n) asm volatile("s_waitcnt lgkmcnt(" #n ")" ::: "memory")
#define BAR() asm volatile("s_barrier" ::: "memory")
#define SCHED() __builtin_amdgcn_sched_barrier(0)
#define PIN1(x) asm volatile("" : "+v"(x))
#define RLX_AGENT __ATOMIC_RELAXED, __HIP_MEMORY_SCOPE_AGENT
#define FINE 0
#define LSTAMP(i) do {} while (0)
#define STAMP(kern, i) do {} while (0)
#define FSTAMP(cond, i) do {} while (0)

__device__ __forceinline__ unsigned cvtpk(float lo, float hi) { f32x2 v = {lo, hi}; bf16x2_t b = __builtin_convertvector(v, bf16x2_t); return __builtin_bit_cast(unsigned, b); }
__device__ __forceinline__ float bf2f(unsigned short b) { return __uint_as_float(((unsigned)b) << 16); }
__device__ __forceinline__ s16x4 trread(LAS const char* p) { return __builtin_bit_cast(s16x4, __builtin_amdgcn_ds_read_tr16_b64_v4i16((LAS v4i16_t*)p)); }
__device__ __forceinline__ bf16x8 cat8(s16x4 lo, s16x4 hi) { return (bf16x8){lo[0], lo[1], lo[2], lo[3], hi[0], hi[1], hi[2], hi[3]}; }
__device__ __forceinline__ void glds16(const char* sbase, unsigned voff, unsigned ldsaddr) {
    asm volatile("s_mov_b32 m0, %2\n\ts_nop 0\n\tglobal_load_lds_dwordx4 %0, %1" :: "v"(voff), "s"(sbase), "s"(ldsaddr) : "memory"); }
__device__ __forceinline__ void glds16_sc1(const char* sbase, unsigned voff, unsigned ldsaddr) {
    asm volatile("s_mov_b32 m0, %2\n\ts_nop 0\n\tglobal_load_lds_dwordx4 %0, %1 sc1" :: "v"(voff), "s"(sbase), "s"(ldsaddr) : "memory"); }
#define LDSP(a) ((LAS const char*)(uintptr_t)(unsigned)(a))
#define CODE_LABEL(name) asm volatile(name ":")
#define CODE_RANGE(base, len, tag, lbeg, lend) do { unsigned long long _pc; int _d; \
    asm volatile("s_getpc_b64 %0\n" tag ":\n\ts_mov_b32 %1, " lbeg "-" tag "\n\ts_mov_b32 %2, " lend "-" lbeg : "=s"(_pc), "=s"(_d), "=s"(len)); \
    base = (const char*)(_pc + (long long)_d); } while (0)
__device__ __forceinline__ float gelu_tanh(float x) {
    const float c0 = -2.0f * 0.7978845608028654f * 1.4426950408889634f, c1 = c0 * 0.044715f;
    const float z = x * __builtin_fmaf(x * x, c1, c0);
    const float e = __builtin_amdgcn_exp2f(z);
    return x * __builtin_amdgcn_rcpf(1.0f + e);
}

constexpr int K1_GW = 2 * 32768 + 2 * 40960;
constexpr int K1_LDS = K1_GW + 2048;
#define K1_ST_AUX 0
constexpr float QSCALE = 0.17677669529663687f * 1.4426950408889634f;

__global__ __launch_bounds__(512, 2) void k1_gemm(const bf16_t* __restrict__ x, const bf16_t* __restrict__ wqkv, const bf16_t* __restrict__ wg,
                                                  const bf16_t* __restrict__ wu, const bf16_t* __restrict__ gate_w, unsigned char* __restrict__ ws) {
    extern __shared__ __attribute__((aligned(16))) char smem[];
    const unsigned lds = (unsigned)(uintptr_t)(LAS char*)smem;
    const int tid = threadIdx.x, wid = __builtin_amdgcn_readfirstlane(tid >> 6), lane = tid & 63;
    const int wr = wid >> 2, wc = wid & 3, fr = lane & 15, fq = lane >> 4;
    const int bid = blockIdx.x, xcd = bid & 7, idx = bid >> 3, ctl = idx & 3, rb = idx >> 2;
    const bool type1 = ctl < 3;
    const int t1 = xcd * 3 + ctl, t2 = xcd;
    const char* gp0; const char* gp1; const char* gp2; const char* gp3; const char* gp4;
    if (type1) {
        gp0 = (const char*)(wg + 128 * t1); gp1 = (const char*)(wg + 128 * t1 + 64); gp2 = (const char*)(wu + 128 * t1); gp3 = (const char*)(wu + 128 * t1 + 64);
        gp4 = (const char*)(wqkv + 64 * t1);
    } else {
        gp0 = (const char*)(wg + 3072 + 64 * t2); gp1 = (const char*)(wu + 3072 + 64 * t2);
        gp2 = (const char*)(wqkv + 64 * (24 + 3 * t2)); gp3 = (const char*)(wqkv + 64 * (24 + 3 * t2 + 1)); gp4 = (const char*)(wqkv + 64 * (24 + 3 * t2 + 2));
    }
    const unsigned ld23 = type1 ? FF * 2 : NQKV * 2;
    const int lr = lane >> 3, ls = lane & 7;
    const unsigned voffA = (unsigned)((8 * wc + lr) * (DM * 2) + ((ls ^ ((4 * (wc & 1) + (lane >> 4)) & 7)) * 16));
    const int sB = 3 * ((lane >> 4) & 1) + 4 * (wc & 1);
    const unsigned voffG = (unsigned)((8 * wc + lr) * (FF * 2) + ((ls ^ sB) * 16));
    const unsigned voffQ = (unsigned)((8 * wc + lr) * (NQKV * 2) + ((ls ^ sB) * 16));
    const unsigned voff23 = type1 ? voffG : voffQ;
    const char* xa = (const char*)x + (size_t)rb * 256 * DM * 2;
#define K1_DMA_A(h, tt, bb) do { const char* _s = xa + (size_t)(h) * 128 * DM * 2 + (size_t)(tt) * 128; \
        _Pragma("unroll") for (int _u = 0; _u < 4; ++_u) glds16(_s + (size_t)_u * 32 * DM * 2, voffA, lds + (bb) * 32768 + ((h) * 128 + _u * 32 + wc * 8) * 128); } while (0)
#define K1_DMA_B(hb, tt, bb) do { const size_t _k = (size_t)(tt) * 64 + (hb) * 32; const unsigned _d = lds + 65536 + (bb) * 40960 + ((hb) * 32 + wc * 8) * 128; \
        glds16(gp0 + _k * (FF * 2), voffG, _d); glds16(gp1 + _k * (FF * 2), voffG, _d + 8192); \
        glds16(gp2 + _k * ld23, voff23, _d + 16384); glds16(gp3 + _k * ld23, voff23, _d + 24576); \
        glds16(gp4 + _k * (NQKV * 2), voffQ, _d + 32768); } while (0)

    STAMP(0, 0);
    const unsigned gscr = lds + 65536 + 40960 + (32 + wc * 8) * 128;
    if (wr == 1) {
        const char* xr = (const char*)x + (size_t)(rb * 256 + (xcd * 4 + ctl) * 8 + wc * 2) * (DM * 2);
#pragma unroll
        for (int i = 0; i < 4; ++i) glds16(xr + i * 1024, (unsigned)(lane * 16), gscr + i * 8192);
    }
    if (wid >= 6) glds16((const char*)gate_w + (wid - 6) * 1024, (unsigned)(lane * 16), lds + K1_GW + (wid - 6) * 1024);
    if (wr == 0) { K1_DMA_B(0, 0, 0); K1_DMA_A(1, 0, 0); K1_DMA_B(0, 1, 1); }
    else         { K1_DMA_A(0, 0, 0); K1_DMA_B(1, 0, 0); K1_DMA_A(0, 1, 1); }

    const unsigned aoff0 = lds + (wr * 128 + fr) * 128 + ((fq ^ (fr >> 1)) * 16);
    const unsigned aoff1 = lds + (wr * 128 + fr) * 128 + (((fq ^ (fr >> 1)) ^ 4) * 16);
    const int q4 = (lane >> 2) & 3, p4 = lane & 3;
    const int sR = 3 * ((q4 >> 1) & 1) + 4 * (fq & 1);
    const int krow = (8 * fq + q4) * 128;
    int gA, cA, gB, cB, gS;
    if (type1) { gA = wc >> 1; cA = 4 * (wc & 1); gB = 2 + (wc >> 1); cB = cA; gS = 4; }
    else if (wc < 2) { gA = 0; cA = 4 * wc; gB = 1; cB = cA; gS = 2; }
    else { gA = wc + 1; cA = 0; gB = wc + 1; cB = 4; gS = 2; }
    const unsigned boffA0 = lds + 65536 + gA * 8192 + krow + (((cA + p4) ^ sR) * 16) + (p4 & 1) * 8;
    const unsigned boffA1 = lds + 65536 + gA * 8192 + krow + (((cA + p4) ^ sR) * 16) + ((p4 & 1) ^ 1) * 8;
    const unsigned boffB0 = lds + 65536 + gB * 8192 + krow + (((cB + p4) ^ sR) * 16) + (p4 & 1) * 8;
    const unsigned boffB1 = lds + 65536 + gB * 8192 + krow + (((cB + p4) ^ sR) * 16) + ((p4 & 1) ^ 1) * 8;
    const unsigned boffS = lds + 65536 + gS * 8192 + krow + (((2 * wc + (p4 >> 1)) ^ sR) * 16) + (p4 & 1) * 8;

    f32x4 acc[8][5];
#pragma unroll
    for (int m = 0; m < 8; ++m)
#pragma unroll
        for (int n = 0; n < 5; ++n) acc[m][n] = (f32x4){0.f, 0.f, 0.f, 0.f};
    bf16x8 af[8], bfg[5];

    WAIT_V(9); BAR();
    STAMP(0, 1);
    if (wr == 1) {
        const u32x4 w0 = *(LAS const u32x4*)(LDSP(lds + K1_GW) + lane * 16), w1 = *(LAS const u32x4*)(LDSP(lds + K1_GW) + 1024 + lane * 16);
#pragma unroll
        for (int r = 0; r < 2; ++r) {
            const u32x4 x0 = *(LAS const u32x4*)(LDSP(gscr) + (2 * r) * 8192 + lane * 16), x1 = *(LAS const u32x4*)(LDSP(gscr) + (2 * r + 1) * 8192 + lane * 16);
            float sacc = 0.f;
#pragma unroll
            for (int i = 0; i < 4; ++i) {
                sacc = __builtin_fmaf(__uint_as_float(x0[i] << 16), __uint_as_float(w0[i] << 16), sacc);
                sacc = __builtin_fmaf(__uint_as_float(x0[i] & 0xffff0000u), __uint_as_float(w0[i] & 0xffff0000u), sacc);
                sacc = __builtin_fmaf(__uint_as_float(x1[i] << 16), __uint_as_float(w1[i] << 16), sacc);
                sacc = __builtin_fmaf(__uint_as_float(x1[i] & 0xffff0000u), __uint_as_float(w1[i] & 0xffff0000u), sacc);
            }
#pragma unroll
            for (int o = 32; o >= 1; o >>= 1) sacc += __shfl_xor(sacc, o);
            if (lane == 0) ((float*)(ws + WS_G))[rb * 256 + (xcd * 4 + ctl) * 8 + wc * 2 + r] = 1.0f / (1.0f + __expf(-sacc));
        }
        WAIT_L(0);
        BAR();
    }

#define K1_SEG(b, ks, t) do { \
        { constexpr int _bo = (b) * 40960 + (ks) * 4096; \
          bfg[0] = cat8(trread(LDSP(boffA0) + _bo), trread(LDSP(boffA0) + _bo + 512)); bfg[1] = cat8(trread(LDSP(boffA1) + _bo), trread(LDSP(boffA1) + _bo + 512)); \
          bfg[2] = cat8(trread(LDSP(boffB0) + _bo), trread(LDSP(boffB0) + _bo + 512)); bfg[3] = cat8(trread(LDSP(boffB1) + _bo), trread(LDSP(boffB1) + _bo + 512)); \
          bfg[4] = cat8(trread(LDSP(boffS) + _bo), trread(LDSP(boffS) + _bo + 512)); \
          LAS const char* _ap = LDSP((ks) ? aoff1 : aoff0) + (b) * 32768; \
          _Pragma("unroll") for (int _m = 0; _m < 8; ++_m) af[_m] = *(LAS const bf16x8*)(_ap + _m * 2048); } \
        if ((ks) == 0) { const int _tt = ((t) + 1 < 16) ? (t) + 1 : 15; if (wr == 0) K1_DMA_A(1, _tt, (b) ^ 1); else K1_DMA_B(1, _tt, (b) ^ 1); } \
        else { const int _tt = ((t) + 2 < 16) ? (t) + 2 : 15; if (wr == 0) K1_DMA_B(0, _tt, (b)); else K1_DMA_A(0, _tt, (b)); } \
        WAIT_V(9); WAIT_L(0); SCHED(); BAR(); SCHED(); \
        __builtin_amdgcn_s_setprio(1); \
        _Pragma("unroll") for (int _m = 0; _m < 8; ++_m) _Pragma("unroll") for (int _n = 0; _n < 5; ++_n) \
            acc[_m][_n] = __builtin_amdgcn_mfma_f32_16x16x32_bf16(bfg[_n], af[_m], acc[_m][_n], 0, 0, 0); \
        __builtin_amdgcn_s_setprio(0); \
        SCHED(); BAR(); SCHED(); } while (0)

    for (int t = 0; t < 16; t += 2) {
        K1_SEG(0, 0, t); K1_SEG(0, 1, t); K1_SEG(1, 0, t + 1); K1_SEG(1, 1, t + 1);
    }
    if (wr == 0) BAR();
    STAMP(0, 2);

    const bool roleP = type1 || wc < 2;
    int lane_e = lane; asm volatile("" : "+v"(lane_e));
    {
    const int fr = lane_e & 15, fq = lane_e >> 4;
    const int row0 = rb * 256 + wr * 128 + fr;
    const bool odd = fq & 1;
    const __amdgpu_buffer_rsrc_t rs = __builtin_amdgcn_make_buffer_rsrc((void*)ws, (short)0, (int)(64u << 20), 0x00020000);
#define K1_QKV_ADDR(col0, dl, voff, step, sc) \
        const int _part = (col0) >> 10, _head = ((col0) & 1023) >> 5, _d = ((col0) & 31) + (dl); \
        const float sc = _part == 0 ? QSCALE : 1.0f; \
        unsigned voff; unsigned step; \
        if (_part == 1) { voff = (unsigned)(WS_K + ((size_t)_head * 65536 + (row0 >> 6) * 2048 + (row0 & 63) * 8) * 2) + (unsigned)(((_d >> 3) * 512 + (_d & 7)) * 2); step = 256u; } \
        else { voff = (unsigned)((_part == 0 ? WS_Q : WS_V) + ((size_t)_head * SEQ) * 64) + (unsigned)(row0 * 64 + _d * 2); step = 1024u; }
#define K1_ROWOFF(part1, m) ((part1) ? (unsigned)(((m) >> 2) * 4096 + ((m) & 3) * 256) : (unsigned)((m) * 1024))
    {
        const int col0 = 64 * (type1 ? t1 : 24 + 3 * t2) + 16 * wc;
        K1_QKV_ADDR(col0, 4 * fq, voff, step, sc);
        const bool isK = step == 256u;
#pragma unroll
        for (int m = 0; m < 8; ++m) {
            const f32x4 v = acc[m][4] * sc;
            u32x2 w; w.x = cvtpk(v[0], v[1]); w.y = cvtpk(v[2], v[3]);
            __builtin_amdgcn_raw_buffer_store_b64(w, rs, voff + (isK ? K1_ROWOFF(true, m) : K1_ROWOFF(false, m)), 0, K1_ST_AUX);
        }
    }
    if (roleP) {
        const int hcol = (type1 ? 128 * t1 : 3072 + 64 * t2) + 32 * wc + 8 * fq;
        const unsigned voff = (unsigned)WS_A + (unsigned)(row0 * (KTOT * 2) + hcol * 2);
#pragma unroll
        for (int m = 0; m < 8; ++m) {
            float h0[4], h1[4];
#pragma unroll
            for (int j = 0; j < 4; ++j) { h0[j] = gelu_tanh(acc[m][0][j]) * acc[m][2][j]; h1[j] = gelu_tanh(acc[m][1][j]) * acc[m][3][j]; }
            const unsigned a0 = cvtpk(h0[0], h0[1]), a1 = cvtpk(h0[2], h0[3]), b0 = cvtpk(h1[0], h1[1]), b1 = cvtpk(h1[2], h1[3]);
            u32x4 w; w.x = odd ? b0 : a0; w.y = odd ? b1 : a1; w.z = odd ? a0 : b0; w.w = odd ? a1 : b1;
            __builtin_amdgcn_raw_buffer_store_b128(w, rs, voff + (unsigned)(m * 16 * KTOT * 2), 0, K1_ST_AUX);
        }
    } else {
#pragma unroll
        for (int pr = 0; pr < 2; ++pr) {
            const int col0 = 64 * (24 + 3 * t2 + (wc - 1)) + 32 * pr;
            K1_QKV_ADDR(col0, 8 * fq, voff, step, sc);
            const bool isK = step == 256u;
#pragma unroll
            for (int m = 0; m < 8; ++m) {
                const f32x4 v0 = acc[m][2 * pr] * sc, v1 = acc[m][2 * pr + 1] * sc;
                const unsigned a0 = cvtpk(v0[0], v0[1]), a1 = cvtpk(v0[2], v0[3]), b0 = cvtpk(v1[0], v1[1]), b1 = cvtpk(v1[2], v1[3]);
                u32x4 w; w.x = odd ? b0 : a0; w.y = odd ? b1 : a1; w.z = odd ? a0 : b0; w.w = odd ? a1 : b1;
                __builtin_amdgcn_raw_buffer_store_b128(w, rs, voff + (isK ? K1_ROWOFF(true, m) : K1_ROWOFF(false, m)), 0, K1_ST_AUX);
            }
        }
    }
#undef K1_QKV_ADDR
#undef K1_ROWOFF
    }
    if (tid == 0) ((unsigned*)(ws + WS_FLAG))[bid * 64] = 0u;
    if (tid == 64 && bid < 32) ((unsigned*)(ws + WS_FLAG))[(256 + bid) * 64] = 0u;
    if (tid >= 128 && tid < 136) ((unsigned*)(ws + WS_XC))[(bid * 8 + (tid - 128)) * 32] = 0u;
    STAMP(0, 3);
    WAIT_V(0);
    STAMP(0, 4);
#undef K1_SEG
#undef K1_DMA_A
#undef K1_DMA_B
}

constexpr int K3_STG = 49152;
constexpr int K23_SCR = 3 * K3_STG;
constexpr int K23_LDS = K23_SCR + 1024;
#define K2_RING(j) (K3_STG + (((j) + 1) % 3) * 32768)
__device__ __forceinline__ int crow(int r, int hi) { return (r & 3) + 8 * (r >> 2) + 4 * hi; }
#define MFMA32(a, b, c) __builtin_amdgcn_mfma_f32_32x32x16_bf16(a, b, c, 0, 0, 0)
#define MFMA16(a, b, c) __builtin_amdgcn_mfma_f32_16x16x32_bf16(a, b, c, 0, 0, 0)
#define MX3(a, b, c) __builtin_fmaxf(__builtin_fmaxf((a), (b)), (c))
#define PIN(x) asm volatile("" : "+v"(x))
#define EX2(v) __builtin_amdgcn_exp2f(v)
__device__ __forceinline__ void glds4_sc1(const char* sbase, unsigned voff, unsigned ldsaddr) {
    asm volatile("s_mov_b32 m0, %2\n\ts_nop 0\n\tglobal_load_lds_dword %0, %1 sc1" :: "v"(voff), "s"(sbase), "s"(ldsaddr) : "memory"); }
__device__ __forceinline__ void glds4(const char* sbase, unsigned voff, unsigned ldsaddr) {
    asm volatile("s_mov_b32 m0, %2\n\ts_nop 0\n\tglobal_load_lds_dword %0, %1" :: "v"(voff), "s"(sbase), "s"(ldsaddr) : "memory"); }

__global__ __launch_bounds__(768, 3) void k23(const bf16_t* __restrict__ wo, const bf16_t* __restrict__ wd, unsigned char* __restrict__ ws, bf16_t* __restrict__ out) {
    extern __shared__ __attribute__((aligned(16))) char smem[];
    const unsigned lds = (unsigned)(uintptr_t)(LAS char*)smem;
    const int tid = threadIdx.x, wid = __builtin_amdgcn_readfirstlane(tid >> 6), lane = tid & 63;
    const int bid = blockIdx.x, xcd = bid & 7, idx = bid >> 3;
    unsigned* flags = (unsigned*)(ws + WS_FLAG);
    const int kq = xcd >> 1, mb = (xcd & 1) * 4 + (idx >> 3), nb = idx & 7;
    const int head = xcd * 4 + (idx & 3), qb = idx >> 2;
    LAS unsigned* fl = (LAS unsigned*)(uintptr_t)(lds + K23_SCR);
    CODE_LABEL("gm_k23_beg");

    if (wid >= 8) {
        const int L = wid - 8;
        const int lr = lane >> 3, ls = lane & 7;
        const char* Kg = (const char*)(ws + WS_K) + (size_t)head * 131072;
        const char* Vg = (const char*)(ws + WS_V) + (size_t)head * 131072;
        const unsigned voff = (unsigned)(L * 4096 + lane * 16);
#define K2_DMA(j) do { const char* _k = Kg + (size_t)(j) * 16384; const char* _v = Vg + (size_t)(j) * 16384; const unsigned _d = lds + K2_RING(j) + L * 4096; \
        _Pragma("unroll") for (int _i = 0; _i < 4; ++_i) glds16(_k + _i * 1024, voff, _d + _i * 1024); \
        _Pragma("unroll") for (int _i = 0; _i < 4; ++_i) glds16(_v + _i * 1024, voff, _d + 16384 + _i * 1024); } while (0)
        const unsigned voffA0 = (unsigned)(lr * (KTOT * 2) + ((ls ^ ((lane >> 4) & 7)) * 16)), voffA1 = (unsigned)(lr * (KTOT * 2) + ((ls ^ ((4 + (lane >> 4)) & 7)) * 16));
        const unsigned voffB0 = (unsigned)(lr * (DM * 2) + ((ls ^ (3 * ((lane >> 4) & 1))) * 16)), voffB1 = (unsigned)(lr * (DM * 2) + ((ls ^ (3 * ((lane >> 4) & 1) + 4)) * 16));
        const char* Ag = (const char*)(ws + WS_A) + (size_t)(mb * 256 + L * 32) * KTOT * 2;
#define K3_DMA_A(tt, h) do { const int _t = (tt) < 18 ? (tt) : 17; const unsigned _d = lds + ((tt) % 3) * K3_STG + (h) * 16384 + L * 4096; \
        const char* _s = Ag + (_t < 14 ? (14 * kq + _t) * 128 : FF * 2 + (4 * kq + _t - 14) * 128) + (size_t)((h) * 128) * KTOT * 2; \
        glds16(_s, voffA0, _d); glds16(_s + (size_t)8 * KTOT * 2, voffA1, _d + 1024); glds16(_s + (size_t)16 * KTOT * 2, voffA0, _d + 2048); glds16(_s + (size_t)24 * KTOT * 2, voffA1, _d + 3072); } while (0)
#define K3_DMA_B(tt, g) do { const int _t = (tt) < 18 ? (tt) : 17; const unsigned _d = lds + ((tt) % 3) * K3_STG + 32768 + (g) * 8192 + L * 2048; \
        const char* _w = (_t < 14 ? (const char*)wd + (size_t)(14 * kq + _t) * 64 * DM * 2 : (const char*)wo + (size_t)(4 * kq + _t - 14) * 64 * DM * 2) + nb * 256 + (g) * 128 + (size_t)(L * 16) * DM * 2; \
        glds16(_w, voffB0, _d); glds16(_w + (size_t)8 * DM * 2, voffB1, _d + 1024); } while (0)
        K2_DMA(0); K2_DMA(1);
        {
            const char* cb; int cl;
            CODE_RANGE(cb, cl, "gm_k23_ref", "gm_k23_beg", "gm_k23_end");
            const int off = ((L & 1) * 64 + lane) * 128;
            if (L >= 2) CODE_RANGE(cb, cl, "gm_k23_ref2", "gm_k23_ref2", "gm_k23_end2");
            if (cl > 0) glds4(cb, (unsigned)(off < cl - 4 ? off : 0), lds + K23_SCR + 256);
        }
        WAIT_V(0); BAR();
#pragma unroll
        for (int j = 0; j < 8; ++j) {
            if (j == 1 || j == 2) { WAIT_V(0); BAR(); } else if (j >= 3 && j <= 5) { WAIT_V(1); BAR(); } else if (j == 6) { WAIT_V(13); BAR(); } else if (j == 7) { BAR(); }
            if (j + 2 < 8) K2_DMA(j + 2);
            if (j >= 2 && j <= 5) {
                const int ins = (j - 2) * 4 + L;
                if (ins < 9) {
                    const int row0 = (idx * 9 + ins) * 4;
                    const char* base = row0 < 896 ? (const char*)wd + (size_t)(896 * kq + row0) * 2048 : (const char*)wo + (size_t)(256 * kq + row0 - 896) * 2048;
                    glds4(base, (unsigned)(lane * 128), lds + K23_SCR + 256);
                } else {
                    const int e = ins < 14 ? ins - 9 : ins - 11;
                    const int x = e * 32 + idx;
                    const char* base = (const char*)(ws + WS_A) + (size_t)((xcd & 1) * 1024 + (x & 15) * 64) * (KTOT * 2) + (14 * kq + (x >> 4)) * 128;
                    glds4(base, (unsigned)(lane * (KTOT * 2)), lds + K23_SCR + 256);
                }
            }
            if (j == 5) { K3_DMA_A(0, 0); K3_DMA_B(0, 0); K3_DMA_B(0, 1); K3_DMA_A(0, 1); }
            if (j == 6) { K3_DMA_A(1, 0); K3_DMA_A(1, 1); }
            if (j == 7) { K3_DMA_B(1, 0); K3_DMA_B(1, 1); }
        }
        BAR(); BAR();
        const unsigned redo = __builtin_amdgcn_readfirstlane(fl[0]);
        if (redo) {
            WAIT_V(0); BAR();
            K2_DMA(0); K2_DMA(1);
            for (int j = 0; j < 8; ++j) {
                if (j < 7) WAIT_V(8); else WAIT_V(0);
                BAR();
                if (j + 2 < 8) K2_DMA(j + 2);
            }
            BAR();
            K3_DMA_A(1, 0); K3_DMA_B(1, 0); K3_DMA_B(1, 1); K3_DMA_A(1, 1);
        }
        WAIT_V(0); BAR();
#define K3_DMA_T(tt) do { K3_DMA_A(tt, 0); K3_DMA_B(tt, 0); K3_DMA_B(tt, 1); K3_DMA_A(tt, 1); } while (0)
        K3_DMA_T(2);
        int ost = 0;
        for (int t = 0; t < 18; ++t) {
            if (L == 0 && t >= 4 && t <= 11) {
                if (t == 8 || t == 10 || t == 11) {
                    WAIT_V(12);
                    if (ost == 0) {
                        if ((unsigned)__builtin_amdgcn_readfirstlane(fl[128]) >= 8u) ost = 2;
                        else if (t == 11) {
                            unsigned spins = 0;
                            while (__builtin_amdgcn_readfirstlane(__hip_atomic_load(flags + (256 + mb * 4 + kq) * 64, RLX_AGENT)) < 8u) { __builtin_amdgcn_s_sleep(1); if (++spins > (1u << 22)) break; }
                            ost = 2;
                        }
                        if (ost == 2 && t == 11) { __builtin_amdgcn_fence(__ATOMIC_ACQUIRE, "agent"); WAIT_V(0); ost = 1; }
                    } else if (t == 11 && ost == 3) WAIT_V(0);
                } else WAIT_V(13);
            } else if (t < 16) WAIT_V(12);
            else WAIT_V(0);
            BAR();
            if (t + 3 < 18) K3_DMA_T(t + 3);
            if (L == 0) {
                if (t == 3) { if (lane == 0) __hip_atomic_fetch_add(flags + (256 + qb * 4 + (xcd >> 1)) * 64, 1u, RLX_AGENT); }
                else if (t == 5 || (t == 8 && ost == 0)) glds4_sc1((const char*)(flags + (256 + mb * 4 + kq) * 64), 0u, lds + K23_SCR + 512);
                else if (ost == 2) { asm volatile("buffer_inv sc1" ::: "memory"); ost = (t == 10) ? 3 : 1; }
            }
        }
#undef K3_DMA_T
        CODE_LABEL("gm_k23_end2");
        return;
#undef K2_DMA
#undef K3_DMA_A
#undef K3_DMA_B
    }

    STAMP(1, 0);
    float omg[4];
    { const float* gv = (const float*)(ws + WS_G) + mb * 256 + (wid >> 2) * 128 + ((wid & 3) >> 1) * 64 + (lane & 15);
#pragma unroll
      for (int m = 0; m < 4; ++m) omg[m] = 1.0f - gv[16 * m]; }

    {
        const int r32 = lane & 31, hi = lane >> 5;
        const bf16_t* Qg = (const bf16_t*)(ws + WS_Q) + ((size_t)head * SEQ + qb * 256 + wid * 32) * 32;
        bf16x8 qf0 = *(const bf16x8*)(Qg + r32 * 32 + hi * 8);
        bf16x8 qf1 = *(const bf16x8*)(Qg + r32 * 32 + 16 + hi * 8);
        float gval = ((const float*)(ws + WS_G))[qb * 256 + wid * 32 + r32];
        bf16x8 ones;
        { const short o1 = (((lane >> 4) & 1) == (((lane & 15) >> 2) & 1)) ? (short)0x3F80 : (short)0;
#pragma unroll
          for (int j = 0; j < 8; ++j) ones[j] = o1; }
        const unsigned koff = lds + hi * 1024 + r32 * 16;
        const unsigned voffr = lds + 16384 + ((lane >> 4) & 1) * 32 + (lane & 3) * 8 + (4 * hi + ((lane & 15) >> 2)) * 64;
        const unsigned koff0 = koff + K2_RING(0);
        f32x16 o = {}; f32x4 lsum = {0.f, 0.f, 0.f, 0.f};
        asm volatile("" : "+v"(omg[0]), "+v"(omg[1]), "+v"(omg[2]), "+v"(omg[3]), "+v"(gval), "+v"(qf0), "+v"(qf1));
        BAR();
        STAMP(1, 1);
        {
            f32x16 SA, SB, negm;
            u32x4 WA0, WA1, WB0 = {0u, 0u, 0u, 0u}, WB1 = {0u, 0u, 0u, 0u};
            bf16x8 kf0, kf1, vf0 = {}, vf1 = {};
            kf0 = *(LAS const bf16x8*)(LDSP(koff0)); kf1 = *(LAS const bf16x8*)(LDSP(koff0) + 2048);
            { f32x16 z = {}; SA = MFMA32(kf0, qf0, z); SA = MFMA32(kf1, qf1, SA); }
            kf0 = *(LAS const bf16x8*)(LDSP(koff0) + 512); kf1 = *(LAS const bf16x8*)(LDSP(koff0) + 2048 + 512);
            {
                float a = MX3(SA[0], SA[1], SA[2]), b = MX3(SA[3], SA[4], SA[5]);
                a = MX3(a, SA[6], SA[7]); b = MX3(b, SA[8], SA[9]); a = MX3(a, SA[10], SA[11]); b = MX3(b, SA[12], SA[13]); a = MX3(a, SA[14], SA[15]);
                float rm = __builtin_fmaxf(a, b);
                auto rr = __builtin_amdgcn_permlane32_swap(__float_as_uint(rm), __float_as_uint(rm), false, false); rm = __builtin_fmaxf(__uint_as_float(rr[0]), __uint_as_float(rr[1]));
#pragma unroll
                for (int r = 0; r < 16; ++r) { SA[r] -= rm; negm[r] = -rm; }
                PIN(negm);
            }
#define K2_STEP(C, N, WP0, WP1, WC0, WC1, KADDR, VADDR) do { \
            N = MFMA32(kf0, qf0, negm); \
            C[0] = EX2(C[0]); C[1] = EX2(C[1]); C[2] = EX2(C[2]); C[3] = EX2(C[3]); PIN(C); SCHED(); \
            kf0 = *(LAS const bf16x8*)(KADDR); SCHED(); \
            o = MFMA32(vf0, __builtin_bit_cast(bf16x8, WP0), o); \
            C[4] = EX2(C[4]); C[5] = EX2(C[5]); C[6] = EX2(C[6]); C[7] = EX2(C[7]); PIN(C); SCHED(); \
            vf0 = cat8(trread(VADDR), trread((VADDR) + 512)); SCHED(); \
            lsum = MFMA16(ones, __builtin_bit_cast(bf16x8, WP0), lsum); \
            WC0[0] = cvtpk(C[0], C[1]); WC0[1] = cvtpk(C[2], C[3]); WC0[2] = cvtpk(C[4], C[5]); WC0[3] = cvtpk(C[6], C[7]); PIN(WC0); SCHED(); \
            N = MFMA32(kf1, qf1, N); \
            C[8] = EX2(C[8]); C[9] = EX2(C[9]); C[10] = EX2(C[10]); C[11] = EX2(C[11]); PIN(C); SCHED(); \
            kf1 = *(LAS const bf16x8*)((KADDR) + 2048); SCHED(); \
            o = MFMA32(vf1, __builtin_bit_cast(bf16x8, WP1), o); \
            C[12] = EX2(C[12]); C[13] = EX2(C[13]); C[14] = EX2(C[14]); C[15] = EX2(C[15]); PIN(C); SCHED(); \
            vf1 = cat8(trread((VADDR) + 1024), trread((VADDR) + 1536)); SCHED(); \
            lsum = MFMA16(ones, __builtin_bit_cast(bf16x8, WP1), lsum); \
            WC1[0] = cvtpk(C[8], C[9]); WC1[1] = cvtpk(C[10], C[11]); WC1[2] = cvtpk(C[12], C[13]); WC1[3] = cvtpk(C[14], C[15]); PIN(WC1); SCHED(); \
            } while (0)
            for (int j = 0; j < 8; ++j) {
                if (j > 0) { WAIT_L(0); BAR(); }
                LAS const char* kc = LDSP(koff + K2_RING(j));
                LAS const char* kn = LDSP(koff + K2_RING(j + 1));
                LAS const char* vc = LDSP(voffr + K2_RING(j));
                K2_STEP(SA, SB, WB0, WB1, WA0, WA1, kc + 1 * 4096, vc + 0 * 2048);
                K2_STEP(SB, SA, WA0, WA1, WB0, WB1, kc + 1 * 4096 + 512, vc + 1 * 2048);
                K2_STEP(SA, SB, WB0, WB1, WA0, WA1, kc + 2 * 4096, vc + 2 * 2048);
                K2_STEP(SB, SA, WA0, WA1, WB0, WB1, kc + 2 * 4096 + 512, vc + 3 * 2048);
                K2_STEP(SA, SB, WB0, WB1, WA0, WA1, kc + 3 * 4096, vc + 4 * 2048);
                K2_STEP(SB, SA, WA0, WA1, WB0, WB1, kc + 3 * 4096 + 512, vc + 5 * 2048);
                K2_STEP(SA, SB, WB0, WB1, WA0, WA1, kn, vc + 6 * 2048);
                K2_STEP(SB, SA, WA0, WA1, WB0, WB1, kn + 512, vc + 7 * 2048);
            }
            o = MFMA32(vf0, __builtin_bit_cast(bf16x8, WB0), o); lsum = MFMA16(ones, __builtin_bit_cast(bf16x8, WB0), lsum);
            o = MFMA32(vf1, __builtin_bit_cast(bf16x8, WB1), o); lsum = MFMA16(ones, __builtin_bit_cast(bf16x8, WB1), lsum);
#undef K2_STEP
        }
        STAMP(1, 2);
        {
            const bool bad = !(lsum[0] <= 1.0e9f);
            if (tid == 0) fl[0] = 0u;
            WAIT_L(0); BAR();
            if (__any(bad) && lane == 0) fl[0] = 1u;
            WAIT_L(0); BAR();
            const unsigned redo = __builtin_amdgcn_readfirstlane(fl[0]);
            if (redo) {
                WAIT_L(0); BAR();
                o = (f32x16){}; lsum = (f32x4){0.f, 0.f, 0.f, 0.f};
                float mhat = 0.f; f32x16 negm = {};
                bool first = true;
                for (int j = 0; j < 8; ++j) {
                    WAIT_L(0); BAR();
                    LAS const char* kb = LDSP(koff + K2_RING(j));
                    LAS const char* vb = LDSP(voffr + K2_RING(j));
#pragma unroll 1
                    for (int tt = 0; tt < 4; ++tt) {
                        LAS const char* kp = kb + tt * 4096;
                        LAS const char* vp = vb + tt * 4096;
                        const bf16x8 k00 = *(LAS const bf16x8*)(kp), k01 = *(LAS const bf16x8*)(kp + 2048), k10 = *(LAS const bf16x8*)(kp + 512), k11 = *(LAS const bf16x8*)(kp + 2048 + 512);
                        f32x16 p0 = MFMA32(k00, qf0, negm); p0 = MFMA32(k01, qf1, p0);
                        f32x16 p1 = MFMA32(k10, qf0, negm); p1 = MFMA32(k11, qf1, p1);
                        float a = MX3(p0[0], p0[1], p1[0]), b = MX3(p0[2], p0[3], p1[1]); a = MX3(a, p1[2], p1[3]);
#pragma unroll
                        for (int r = 4; r < 16; r += 4) { a = MX3(a, p0[r], p0[r + 1]); b = MX3(b, p0[r + 2], p0[r + 3]); a = MX3(a, p1[r], p1[r + 1]); b = MX3(b, p1[r + 2], p1[r + 3]); }
                        float rm = __builtin_fmaxf(a, b);
                        { auto rr = __builtin_amdgcn_permlane32_swap(__float_as_uint(rm), __float_as_uint(rm), false, false); rm = __builtin_fmaxf(__uint_as_float(rr[0]), __uint_as_float(rr[1])); }
                        {
                            const float dl = first ? rm : __builtin_fmaxf(rm, 0.f);
                            mhat += dl;
#pragma unroll
                            for (int r = 0; r < 16; ++r) { p0[r] -= dl; p1[r] -= dl; negm[r] = -mhat; }
                            if (!first) { const float f = EX2(-dl); o = o * f; lsum = lsum * f; }
                            first = false;
                        }
#pragma unroll
                        for (int r = 0; r < 16; ++r) { p0[r] = EX2(p0[r]); p1[r] = EX2(p1[r]); }
                        const u32x4 w0 = {cvtpk(p0[0], p0[1]), cvtpk(p0[2], p0[3]), cvtpk(p0[4], p0[5]), cvtpk(p0[6], p0[7])};
                        const u32x4 w1 = {cvtpk(p0[8], p0[9]), cvtpk(p0[10], p0[11]), cvtpk(p0[12], p0[13]), cvtpk(p0[14], p0[15])};
                        const u32x4 w2 = {cvtpk(p1[0], p1[1]), cvtpk(p1[2], p1[3]), cvtpk(p1[4], p1[5]), cvtpk(p1[6], p1[7])};
                        const u32x4 w3 = {cvtpk(p1[8], p1[9]), cvtpk(p1[10], p1[11]), cvtpk(p1[12], p1[13]), cvtpk(p1[14], p1[15])};
                        const bf16x8 v0 = cat8(trread(vp), trread(vp + 512)), v1 = cat8(trread(vp + 1024), trread(vp + 1536));
                        const bf16x8 v2 = cat8(trread(vp + 2048), trread(vp + 2560)), v3 = cat8(trread(vp + 3072), trread(vp + 3584));
                        const bf16x8 f0 = __builtin_bit_cast(bf16x8, w0), f1 = __builtin_bit_cast(bf16x8, w1), f2 = __builtin_bit_cast(bf16x8, w2), f3 = __builtin_bit_cast(bf16x8, w3);
                        o = MFMA32(v0, f0, o); lsum = MFMA16(ones, f0, lsum);
                        o = MFMA32(v1, f1, o); lsum = MFMA16(ones, f1, lsum);
                        o = MFMA32(v2, f2, o); lsum = MFMA16(ones, f2, lsum);
                        o = MFMA32(v3, f3, o); lsum = MFMA16(ones, f3, lsum);
                    }
                }
                WAIT_L(0); BAR();
            }
        }
        {
            const float sc = gval * __builtin_amdgcn_rcpf(lsum[0]);
            unsigned w[8];
#pragma unroll
            for (int g = 0; g < 4; ++g) { w[2 * g] = cvtpk(o[4 * g] * sc, o[4 * g + 1] * sc); w[2 * g + 1] = cvtpk(o[4 * g + 2] * sc, o[4 * g + 3] * sc); }
            const __amdgpu_buffer_rsrc_t rs = __builtin_amdgcn_make_buffer_rsrc((void*)(ws + WS_A), (short)0, (int)(SEQ * KTOT * 2), 0x00020000);
            const unsigned ob = (unsigned)(((qb * 256 + wid * 32 + r32) * KTOT + FF + head * 32 + 8 * hi) * 2);
#pragma unroll
            for (int pr = 0; pr < 2; ++pr) {
                auto r0 = __builtin_amdgcn_permlane32_swap(w[4 * pr], w[4 * pr + 2], false, false);
                auto r1 = __builtin_amdgcn_permlane32_swap(w[4 * pr + 1], w[4 * pr + 3], false, false);
                const u32x4 v = {r0[0], r1[0], r0[1], r1[1]};
                __builtin_amdgcn_raw_buffer_store_b128(v, rs, ob + pr * 32, 0, 16);
            }
            STAMP(1, 3);
            WAIT_L(0); BAR();
            STAMP(1, 4);
        }
    }

    {
        const int G = wid >> 2, j4 = wid & 3, wm = j4 >> 1, wn = j4 & 1, fr = lane & 15, fq = lane >> 4;
        const int arow = G * 128 + wm * 64 + fr;
        const unsigned aoff0 = lds + arow * 128 + ((fq ^ (fr >> 1)) * 16);
        const unsigned aoff1 = lds + arow * 128 + (((fq ^ (fr >> 1)) ^ 4) * 16);
        const int q4 = (lane >> 2) & 3, p4 = lane & 3;
        const int sR = 3 * ((q4 >> 1) & 1) + 4 * (fq & 1);
        const int krow = (8 * fq + q4) * 128;
        const unsigned boff00 = lds + 32768 + wn * 8192 + krow + ((p4 ^ sR) * 16) + (p4 & 1) * 8;
        const unsigned boff01 = lds + 32768 + wn * 8192 + krow + ((p4 ^ sR) * 16) + ((p4 & 1) ^ 1) * 8;
        const unsigned boff10 = lds + 32768 + wn * 8192 + krow + (((4 + p4) ^ sR) * 16) + (p4 & 1) * 8;
        const unsigned boff11 = lds + 32768 + wn * 8192 + krow + (((4 + p4) ^ sR) * 16) + ((p4 & 1) ^ 1) * 8;
        f32x4 acc[4][4];
#pragma unroll
        for (int m = 0; m < 4; ++m)
#pragma unroll
            for (int n = 0; n < 4; ++n) acc[m][n] = (f32x4){0.f, 0.f, 0.f, 0.f};
        unsigned fts = 0;
        STAMP(2, 0);
        STAMP(2, 1);
#define K3_READ(F_A, F_B, so_, ks_) do { \
            F_B[0] = cat8(trread(LDSP(boff00 + (so_)) + (ks_) * 4096), trread(LDSP(boff00 + (so_)) + (ks_) * 4096 + 512)); \
            F_A[0] = *(LAS const bf16x8*)(LDSP(((ks_) ? aoff1 : aoff0) + (so_))); \
            F_B[1] = cat8(trread(LDSP(boff01 + (so_)) + (ks_) * 4096), trread(LDSP(boff01 + (so_)) + (ks_) * 4096 + 512)); \
            F_A[1] = *(LAS const bf16x8*)(LDSP(((ks_) ? aoff1 : aoff0) + (so_)) + 2048); \
            F_B[2] = cat8(trread(LDSP(boff10 + (so_)) + (ks_) * 4096), trread(LDSP(boff10 + (so_)) + (ks_) * 4096 + 512)); \
            F_A[2] = *(LAS const bf16x8*)(LDSP(((ks_) ? aoff1 : aoff0) + (so_)) + 4096); \
            F_B[3] = cat8(trread(LDSP(boff11 + (so_)) + (ks_) * 4096), trread(LDSP(boff11 + (so_)) + (ks_) * 4096 + 512)); \
            F_A[3] = *(LAS const bf16x8*)(LDSP(((ks_) ? aoff1 : aoff0) + (so_)) + 6144); } while (0)
#define K3_RB(dst, boff, so_, ks_) dst = cat8(trread(LDSP((boff) + (so_)) + (ks_) * 4096), trread(LDSP((boff) + (so_)) + (ks_) * 4096 + 512))
#define K3_RA(dst, so_, ks_, m_) dst = *(LAS const bf16x8*)(LDSP(((ks_) ? aoff1 : aoff0) + (so_)) + (m_) * 2048)
#define K3_M(CA, CB, m_, n_) acc[m_][n_] = MFMA16(CB[n_], CA[m_], acc[m_][n_])
#define K3_STEP(CA, CB, NA, NB, so_, ks_) do { \
            K3_M(CA, CB, 0, 0); K3_RA(NA[0], so_, ks_, 0); SCHED(); \
            K3_M(CA, CB, 0, 1); K3_RB(NB[0], boff00, so_, ks_); SCHED(); \
            K3_M(CA, CB, 0, 2); SCHED(); \
            K3_M(CA, CB, 0, 3); K3_RB(NB[1], boff01, so_, ks_); SCHED(); \
            K3_M(CA, CB, 1, 0); SCHED(); \
            K3_M(CA, CB, 1, 1); K3_RB(NB[2], boff10, so_, ks_); SCHED(); \
            K3_M(CA, CB, 1, 2); SCHED(); \
            K3_M(CA, CB, 1, 3); K3_RB(NB[3], boff11, so_, ks_); SCHED(); \
            K3_M(CA, CB, 2, 0); SCHED(); \
            K3_M(CA, CB, 2, 1); K3_RA(NA[1], so_, ks_, 1); SCHED(); \
            K3_M(CA, CB, 2, 2); K3_RA(NA[2], so_, ks_, 2); SCHED(); \
            K3_M(CA, CB, 2, 3); K3_RA(NA[3], so_, ks_, 3); SCHED(); \
            K3_M(CA, CB, 3, 0); K3_M(CA, CB, 3, 1); K3_M(CA, CB, 3, 2); K3_M(CA, CB, 3, 3); SCHED(); } while (0)
        bf16x8 a0[4], b0[4], a1[4], b1[4];
        K3_READ(a0, b0, 0u, 0);
        for (int t = 0; t < 18; ++t) {
            const unsigned so = (t % 3) * K3_STG, sn = ((t + 1) % 3) * K3_STG;
            if (t == 14) {
#pragma unroll
                for (int m = 0; m < 4; ++m)
#pragma unroll
                    for (int n = 0; n < 4; ++n) acc[m][n] = acc[m][n] * omg[m];
            }
            SCHED();
            K3_STEP(a0, b0, a1, b1, so, 1);
            if (t == 3) WAIT_V(0);
            WAIT_L(0); SCHED(); BAR(); SCHED();
            K3_STEP(a1, b1, a0, b0, sn, 0);
        }
#undef K3_STEP
#undef K3_M
#undef K3_RA
#undef K3_RB
#undef K3_READ
        STAMP(2, 2);
        {
            const __amdgpu_buffer_rsrc_t rs = __builtin_amdgcn_make_buffer_rsrc((void*)(ws + WS_P), (short)0, (int)(256 * 65536), 0x00020000);
            const unsigned so = (unsigned)(bid * 65536 + wid * 2048 + lane * 16);
            f32x4 fin[4];
#define K3_SEND(q) do { \
                const u32x4 w0 = {cvtpk(acc[q][0][0], acc[q][0][1]), cvtpk(acc[q][0][2], acc[q][0][3]), cvtpk(acc[q][1][0], acc[q][1][1]), cvtpk(acc[q][1][2], acc[q][1][3])}; \
                const u32x4 w1 = {cvtpk(acc[q][2][0], acc[q][2][1]), cvtpk(acc[q][2][2], acc[q][2][3]), cvtpk(acc[q][3][0], acc[q][3][1]), cvtpk(acc[q][3][2], acc[q][3][3])}; \
                __builtin_amdgcn_raw_buffer_store_b128(w0, rs, so + (q) * 16384, 0, 16); __builtin_amdgcn_raw_buffer_store_b128(w1, rs, so + (q) * 16384 + 1024, 0, 16); } while (0)
#define K3_KEEP(q) do { _Pragma("unroll") for (int n = 0; n < 4; ++n) fin[n] = acc[q][n]; } while (0)
            if (kq == 0) { K3_SEND(1); K3_SEND(2); K3_SEND(3); K3_KEEP(0); }
            else if (kq == 1) { K3_SEND(0); K3_SEND(2); K3_SEND(3); K3_KEEP(1); }
            else if (kq == 2) { K3_SEND(0); K3_SEND(1); K3_SEND(3); K3_KEEP(2); }
            else { K3_SEND(0); K3_SEND(1); K3_SEND(2); K3_KEEP(3); }
#undef K3_SEND
#undef K3_KEEP
            STAMP(2, 3);
            WAIT_V(0);
            STAMP(2, 4);
            unsigned* xcnt = (unsigned*)(ws + WS_XC);
            if (lane < 3) { const int q = (kq + 1 + lane) & 3; __hip_atomic_fetch_add(xcnt + ((idx * 8 + 2 * q + (xcd & 1)) * 8 + wid) * 32, 1u, RLX_AGENT); }
            {
                unsigned spins = 0;
                while (__builtin_amdgcn_readfirstlane(__hip_atomic_load(xcnt + (bid * 8 + wid) * 32, RLX_AGENT)) < 3u) { __builtin_amdgcn_s_sleep(1); if (++spins > (1u << 22)) break; }
            }
            asm volatile("" ::: "memory");
            STAMP(2, 5);
            u32x4 rv[3][2];
#pragma unroll
            for (int i = 0; i < 3; ++i) {
                const int q = (kq + 1 + i) & 3;
                const unsigned po = (unsigned)((idx * 8 + 2 * q + (xcd & 1)) * 65536 + kq * 16384 + wid * 2048 + lane * 16);
                rv[i][0] = __builtin_amdgcn_raw_buffer_load_b128(rs, po, 0, 16); rv[i][1] = __builtin_amdgcn_raw_buffer_load_b128(rs, po + 1024, 0, 16);
            }
#pragma unroll
            for (int i = 0; i < 3; ++i)
#pragma unroll
                for (int n = 0; n < 4; ++n) {
                    const unsigned u0 = rv[i][n >> 1][(n & 1) * 2], u1 = rv[i][n >> 1][(n & 1) * 2 + 1];
                    fin[n][0] += __uint_as_float(u0 << 16); fin[n][1] += __uint_as_float(u0 & 0xffff0000u);
                    fin[n][2] += __uint_as_float(u1 << 16); fin[n][3] += __uint_as_float(u1 & 0xffff0000u);
                }
            const int row = mb * 256 + G * 128 + wm * 64 + kq * 16 + fr;
            const bool odd = fq & 1;
#pragma unroll
            for (int pr = 0; pr < 2; ++pr) {
                const f32x4 v0 = fin[2 * pr], v1 = fin[2 * pr + 1];
                const unsigned a0 = cvtpk(v0[0], v0[1]), a1 = cvtpk(v0[2], v0[3]), b0 = cvtpk(v1[0], v1[1]), b1 = cvtpk(v1[2], v1[3]);
                u32x4 w; w.x = odd ? b0 : a0; w.y = odd ? b1 : a1; w.z = odd ? a0 : b0; w.w = odd ? a1 : b1;
                *(u32x4*)(out + (size_t)row * DM + nb * 128 + wn * 64 + pr * 32 + 8 * fq) = w;
            }
        }
        STAMP(2, 6);
        WAIT_V(0);
        STAMP(2, 7);
    }
    CODE_LABEL("gm_k23_end");
}

extern "C" void kernel_launch(void* const* d_in, const int* in_sizes, int n_in, void* d_out, int out_size, void* d_ws, size_t ws_size, hipStream_t stream) {
    const bf16_t* x = (const bf16_t*)d_in[0];
    const bf16_t* wqkv = (const bf16_t*)d_in[1];
    const bf16_t* wo = (const bf16_t*)d_in[2];
    const bf16_t* wg = (const bf16_t*)d_in[4];
    const bf16_t* wu = (const bf16_t*)d_in[5];
    const bf16_t* wd = (const bf16_t*)d_in[6];
    const bf16_t* gw = (const bf16_t*)d_in[7];
    unsigned char* ws = (unsigned char*)d_ws;
    (void)hipFuncSetAttribute((const void*)k1_gemm, hipFuncAttributeMaxDynamicSharedMemorySize, K1_LDS);
    (void)hipFuncSetAttribute((const void*)k23, hipFuncAttributeMaxDynamicSharedMemorySize, K23_LDS);
    k1_gemm<<<256, 512, K1_LDS, stream>>>(x, wqkv, wg, wu, gw, ws);
    k23<<<256, 768, K23_LDS, stream>>>(wo, wd, ws, (bf16_t*)d_out);
}
```

```cpp
#include <hip/hip_runtime.h>
#include <hip/hip_bf16.h>
#include <stdint.h>

#define LAS __attribute__((address_space(3)))
typedef unsigned short bf16_t;
typedef short bf16x8 __attribute__((ext_vector_type(8)));
typedef short s16x4 __attribute__((ext_vector_type(4)));
typedef short v4i16_t __attribute__((ext_vector_type(4)));
typedef float f32x2 __attribute__((ext_vector_type(2)));
typedef float f32x4 __attribute__((ext_vector_type(4)));
typedef float f32x16 __attribute__((ext_vector_type(16)));
typedef unsigned u32x2 __attribute__((ext_vector_type(2)));
typedef unsigned u32x4 __attribute__((ext_vector_type(4)));
typedef __bf16 bf16x2_t __attribute__((ext_vector_type(2)));

constexpr int SEQ = 2048, DM = 1024, NQKV = 3072, FF = 3584, NH = 32, DH = 32;
constexpr int KTOT = FF + DM;
constexpr size_t WS_FLAG = 0;
constexpr size_t WS_G = 131072;
constexpr size_t WS_XC = 262144;
constexpr size_t WS_Q = 1u << 20;
constexpr size_t WS_K = WS_Q + (4u << 20);
constexpr size_t WS_V = WS_K + (4u << 20);
constexpr size_t WS_A = WS_V + (4u << 20);
constexpr size_t WS_P = WS_A + (20u << 20);

#define WAIT_V(n) asm volatile("s_waitcnt vmcnt(" #n ")" ::: "memory")
#define WAIT_L(n) asm volatile("s_waitcnt lgkmcnt(" #n ")" ::: "memory")
#define BAR() asm volatile("s_barrier" ::: "memory")
#define SCHED() __builtin_amdgcn_sched_barrier(0)
#define PIN1(x) asm volatile("" : "+v"(x))
#define RLX_AGENT __ATOMIC_RELAXED, __HIP_MEMORY_SCOPE_AGENT
#define FINE 0
#define LSTAMP(i) do {} while (0)
#define STAMP(kern, i) do {} while (0)
#define FSTAMP(cond, i) do {} while (0)

__device__ __forceinline__ unsigned cvtpk(float lo, float hi) { f32x2 v = {lo, hi}; bf16x2_t b = __builtin_convertvector(v, bf16x2_t); return __builtin_bit_cast(unsigned, b); }
__device__ __forceinline__ float bf2f(unsigned short b) { return __uint_as_float(((unsigned)b) << 16); }
__device__ __forceinline__ s16x4 trread(LAS const char* p) { return __builtin_bit_cast(s16x4, __builtin_amdgcn_ds_read_tr16_b64_v4i16((LAS v4i16_t*)p)); }
__device__ __forceinline__ bf16x8 cat8(s16x4 lo, s16x4 hi) { return (bf16x8){lo[0], lo[1], lo[2], lo[3], hi[0], hi[1], hi[2], hi[3]}; }
__device__ __forceinline__ void glds16(const char* sbase, unsigned voff, unsigned ldsaddr) {
    asm volatile("s_mov_b32 m0, %2\n\ts_nop 0\n\tglobal_load_lds_dwordx4 %0, %1" :: "v"(voff), "s"(sbase), "s"(ldsaddr) : "memory"); }
__device__ __forceinline__ void glds16_sc1(const char* sbase, unsigned voff, unsigned ldsaddr) {
    asm volatile("s_mov_b32 m0, %2\n\ts_nop 0\n\tglobal_load_lds_dwordx4 %0, %1 sc1" :: "v"(voff), "s"(sbase), "s"(ldsaddr) : "memory"); }
#define LDSP(a) ((LAS const char*)(uintptr_t)(unsigned)(a))
#define CODE_LABEL(name) asm volatile(name ":")
#define CODE_RANGE(base, len, tag, lbeg, lend) do { unsigned long long _pc; int _d; \
    asm volatile("s_getpc_b64 %0\n" tag ":\n\ts_mov_b32 %1, " lbeg "-" tag "\n\ts_mov_b32 %2, " lend "-" lbeg : "=s"(_pc), "=s"(_d), "=s"(len)); \
    base = (const char*)(_pc + (long long)_d); } while (0)
__device__ __forceinline__ float gelu_tanh(float x) {
    const float c0 = -2.0f * 0.7978845608028654f * 1.4426950408889634f, c1 = c0 * 0.044715f;
    const float z = x * __builtin_fmaf(x * x, c1, c0);
    const float e = __builtin_amdgcn_exp2f(z);
    return x * __builtin_amdgcn_rcpf(1.0f + e);
}

__device__ __forceinline__ f32x2 geglu2(f32x2 g, f32x2 u) {
    const float c0 = -2.0f * 0.7978845608028654f * 1.4426950408889634f, c1 = c0 * 0.044715f;
    const f32x2 t = __builtin_elementwise_fma(g * g, (f32x2){c1, c1}, (f32x2){c0, c0});
    const f32x2 z = g * t;
    f32x2 e; e.x = __builtin_amdgcn_exp2f(z.x); e.y = __builtin_amdgcn_exp2f(z.y);
    const f32x2 d = e + (f32x2){1.0f, 1.0f};
    f32x2 r; r.x = __builtin_amdgcn_rcpf(d.x); r.y = __builtin_amdgcn_rcpf(d.y);
    return (g * u) * r;
}

constexpr int K1_GW = 2 * 32768 + 2 * 40960;
constexpr int K1_LDS = K1_GW + 2048;
#define K1_ST_AUX 0
constexpr float QSCALE = 0.17677669529663687f * 1.4426950408889634f;

__global__ __launch_bounds__(512, 2) void k1_gemm(const bf16_t* __restrict__ x, const bf16_t* __restrict__ wqkv, const bf16_t* __restrict__ wg,
                                                  const bf16_t* __restrict__ wu, const bf16_t* __restrict__ gate_w, unsigned char* __restrict__ ws) {
    extern __shared__ __attribute__((aligned(16))) char smem[];
    const unsigned lds = (unsigned)(uintptr_t)(LAS char*)smem;
    const int tid = threadIdx.x, wid = __builtin_amdgcn_readfirstlane(tid >> 6), lane = tid & 63;
    const int wr = wid >> 2, wc = wid & 3, fr = lane & 15, fq = lane >> 4;
    const int bid = blockIdx.x, xcd = bid & 7, idx = bid >> 3, ctl = idx & 3, rb = idx >> 2;
    const bool type1 = ctl < 3;
    const int t1 = xcd * 3 + ctl, t2 = xcd;
    const char* gp0; const char* gp1; const char* gp2; const char* gp3; const char* gp4;
    if (type1) {
        gp0 = (const char*)(wg + 128 * t1); gp1 = (const char*)(wg + 128 * t1 + 64); gp2 = (const char*)(wu + 128 * t1); gp3 = (const char*)(wu + 128 * t1 + 64);
        gp4 = (const char*)(wqkv + 64 * t1);
    } else {
        gp0 = (const char*)(wg + 3072 + 64 * t2); gp1 = (const char*)(wu + 3072 + 64 * t2);
        gp2 = (const char*)(wqkv + 64 * (24 + 3 * t2)); gp3 = (const char*)(wqkv + 64 * (24 + 3 * t2 + 1)); gp4 = (const char*)(wqkv + 64 * (24 + 3 * t2 + 2));
    }
    const unsigned ld23 = type1 ? FF * 2 : NQKV * 2;
    const int lr = lane >> 3, ls = lane & 7;
    const unsigned voffA = (unsigned)((8 * wc + lr) * (DM * 2) + ((ls ^ ((4 * (wc & 1) + (lane >> 4)) & 7)) * 16));
    const int sB = 3 * ((lane >> 4) & 1) + 4 * (wc & 1);
    const unsigned voffG = (unsigned)((8 * wc + lr) * (FF * 2) + ((ls ^ sB) * 16));
    const unsigned voffQ = (unsigned)((8 * wc + lr) * (NQKV * 2) + ((ls ^ sB) * 16));
    const unsigned voff23 = type1 ? voffG : voffQ;
    const char* xa = (const char*)x + (size_t)rb * 256 * DM * 2;
#define K1_DMA_A(h, tt, bb) do { const char* _s = xa + (size_t)(h) * 128 * DM * 2 + (size_t)(tt) * 128; \
        _Pragma("unroll") for (int _u = 0; _u < 4; ++_u) glds16(_s + (size_t)_u * 32 * DM * 2, voffA, lds + (bb) * 32768 + ((h) * 128 + _u * 32 + wc * 8) * 128); } while (0)
#define K1_DMA_B(hb, tt, bb) do { const size_t _k = (size_t)(tt) * 64 + (hb) * 32; const unsigned _d = lds + 65536 + (bb) * 40960 + ((hb) * 32 + wc * 8) * 128; \
        glds16(gp0 + _k * (FF * 2), voffG, _d); glds16(gp1 + _k * (FF * 2), voffG, _d + 8192); \
        glds16(gp2 + _k * ld23, voff23, _d + 16384); glds16(gp3 + _k * ld23, voff23, _d + 24576); \
        glds16(gp4 + _k * (NQKV * 2), voffQ, _d + 32768); } while (0)

    STAMP(0, 0);
    const unsigned gscr = lds + 65536 + 40960 + (32 + wc * 8) * 128;
    if (wr == 1) {
        const char* xr = (const char*)x + (size_t)(rb * 256 + (xcd * 4 + ctl) * 8 + wc * 2) * (DM * 2);
#pragma unroll
        for (int i = 0; i < 4; ++i) glds16(xr + i * 1024, (unsigned)(lane * 16), gscr + i * 8192);
    }
    if (wid >= 6) glds16((const char*)gate_w + (wid - 6) * 1024, (unsigned)(lane * 16), lds + K1_GW + (wid - 6) * 1024);
    if (wr == 0) { K1_DMA_B(0, 0, 0); K1_DMA_A(1, 0, 0); K1_DMA_B(0, 1, 1); }
    else         { K1_DMA_A(0, 0, 0); K1_DMA_B(1, 0, 0); K1_DMA_A(0, 1, 1); }

    const unsigned aoff0 = lds + (wr * 128 + fr) * 128 + ((fq ^ (fr >> 1)) * 16);
    const unsigned aoff1 = lds + (wr * 128 + fr) * 128 + (((fq ^ (fr >> 1)) ^ 4) * 16);
    const int q4 = (lane >> 2) & 3, p4 = lane & 3;
    const int sR = 3 * ((q4 >> 1) & 1) + 4 * (fq & 1);
    const int krow = (8 * fq + q4) * 128;
    int gA, cA, gB, cB, gS;
    if (type1) { gA = wc >> 1; cA = 4 * (wc & 1); gB = 2 + (wc >> 1); cB = cA; gS = 4; }
    else if (wc < 2) { gA = 0; cA = 4 * wc; gB = 1; cB = cA; gS = 2; }
    else { gA = wc + 1; cA = 0; gB = wc + 1; cB = 4; gS = 2; }
    const unsigned boffA0 = lds + 65536 + gA * 8192 + krow + (((cA + p4) ^ sR) * 16) + (p4 & 1) * 8;
    const unsigned boffA1 = lds + 65536 + gA * 8192 + krow + (((cA + p4) ^ sR) * 16) + ((p4 & 1) ^ 1) * 8;
    const unsigned boffB0 = lds + 65536 + gB * 8192 + krow + (((cB + p4) ^ sR) * 16) + (p4 & 1) * 8;
    const unsigned boffB1 = lds + 65536 + gB * 8192 + krow + (((cB + p4) ^ sR) * 16) + ((p4 & 1) ^ 1) * 8;
    const unsigned boffS = lds + 65536 + gS * 8192 + krow + (((2 * wc + (p4 >> 1)) ^ sR) * 16) + (p4 & 1) * 8;

    f32x4 acc[8][5];
#pragma unroll
    for (int m = 0; m < 8; ++m)
#pragma unroll
        for (int n = 0; n < 5; ++n) acc[m][n] = (f32x4){0.f, 0.f, 0.f, 0.f};
    bf16x8 af[8], bfg[5];

    WAIT_V(9); BAR();
    STAMP(0, 1);
    if (wr == 1) {
        const u32x4 w0 = *(LAS const u32x4*)(LDSP(lds + K1_GW) + lane * 16), w1 = *(LAS const u32x4*)(LDSP(lds + K1_GW) + 1024 + lane * 16);
#pragma unroll
        for (int r = 0; r < 2; ++r) {
            const u32x4 x0 = *(LAS const u32x4*)(LDSP(gscr) + (2 * r) * 8192 + lane * 16), x1 = *(LAS const u32x4*)(LDSP(gscr) + (2 * r + 1) * 8192 + lane * 16);
            float sacc = 0.f;
#pragma unroll
            for (int i = 0; i < 4; ++i) {
                sacc = __builtin_fmaf(__uint_as_float(x0[i] << 16), __uint_as_float(w0[i] << 16), sacc);
                sacc = __builtin_fmaf(__uint_as_float(x0[i] & 0xffff0000u), __uint_as_float(w0[i] & 0xffff0000u), sacc);
                sacc = __builtin_fmaf(__uint_as_float(x1[i] << 16), __uint_as_float(w1[i] << 16), sacc);
                sacc = __builtin_fmaf(__uint_as_float(x1[i] & 0xffff0000u), __uint_as_float(w1[i] & 0xffff0000u), sacc);
            }
#pragma unroll
            for (int o = 32; o >= 1; o >>= 1) sacc += __shfl_xor(sacc, o);
            if (lane == 0) ((float*)(ws + WS_G))[rb * 256 + (xcd * 4 + ctl) * 8 + wc * 2 + r] = 1.0f / (1.0f + __expf(-sacc));
        }
        WAIT_L(0);
        BAR();
    }

#define K1_SEG(b, ks, t) do { \
        { constexpr int _bo = (b) * 40960 + (ks) * 4096; \
          bfg[0] = cat8(trread(LDSP(boffA0) + _bo), trread(LDSP(boffA0) + _bo + 512)); bfg[1] = cat8(trread(LDSP(boffA1) + _bo), trread(LDSP(boffA1) + _bo + 512)); \
          bfg[2] = cat8(trread(LDSP(boffB0) + _bo), trread(LDSP(boffB0) + _bo + 512)); bfg[3] = cat8(trread(LDSP(boffB1) + _bo), trread(LDSP(boffB1) + _bo + 512)); \
          bfg[4] = cat8(trread(LDSP(boffS) + _bo), trread(LDSP(boffS) + _bo + 512)); \
          LAS const char* _ap = LDSP((ks) ? aoff1 : aoff0) + (b) * 32768; \
          _Pragma("unroll") for (int _m = 0; _m < 8; ++_m) af[_m] = *(LAS const bf16x8*)(_ap + _m * 2048); } \
        if ((ks) == 0) { const int _tt = ((t) + 1 < 16) ? (t) + 1 : 15; if (wr == 0) K1_DMA_A(1, _tt, (b) ^ 1); else K1_DMA_B(1, _tt, (b) ^ 1); } \
        else { const int _tt = ((t) + 2 < 16) ? (t) + 2 : 15; if (wr == 0) K1_DMA_B(0, _tt, (b)); else K1_DMA_A(0, _tt, (b)); } \
        WAIT_V(9); WAIT_L(0); SCHED(); BAR(); SCHED(); \
        __builtin_amdgcn_s_setprio(1); \
        _Pragma("unroll") for (int _m = 0; _m < 8; ++_m) _Pragma("unroll") for (int _n = 0; _n < 5; ++_n) \
            acc[_m][_n] = __builtin_amdgcn_mfma_f32_16x16x32_bf16(bfg[_n], af[_m], acc[_m][_n], 0, 0, 0); \
        __builtin_amdgcn_s_setprio(0); \
        SCHED(); if (!((t) == 15 && (ks) == 1 && wr == 1)) BAR(); SCHED(); } while (0)

    for (int t = 0; t < 16; t += 2) {
        K1_SEG(0, 0, t); K1_SEG(0, 1, t); K1_SEG(1, 0, t + 1); K1_SEG(1, 1, t + 1);
    }
    STAMP(0, 2);

    const bool roleP = type1 || wc < 2;
    int lane_e = lane; asm volatile("" : "+v"(lane_e));
    {
    const int fr = lane_e & 15, fq = lane_e >> 4;
    const int row0 = rb * 256 + wr * 128 + fr;
    const bool odd = fq & 1;
    const __amdgpu_buffer_rsrc_t rs = __builtin_amdgcn_make_buffer_rsrc((void*)ws, (short)0, (int)(64u << 20), 0x00020000);
#define K1_QKV_ADDR(col0, dl, voff, step, sc) \
        const int _part = (col0) >> 10, _head = ((col0) & 1023) >> 5, _d = ((col0) & 31) + (dl); \
        const float sc = _part == 0 ? QSCALE : 1.0f; \
        unsigned voff; unsigned step; \
        if (_part == 1) { voff = (unsigned)(WS_K + ((size_t)_head * 65536 + (row0 >> 6) * 2048 + (row0 & 63) * 8) * 2) + (unsigned)(((_d >> 3) * 512 + (_d & 7)) * 2); step = 256u; } \
        else { voff = (unsigned)((_part == 0 ? WS_Q : WS_V) + ((size_t)_head * SEQ) * 64) + (unsigned)(row0 * 64 + _d * 2); step = 1024u; }
#define K1_ROWOFF(part1, m) ((part1) ? (unsigned)(((m) >> 2) * 4096 + ((m) & 3) * 256) : (unsigned)((m) * 1024))
    {
        const int col0 = 64 * (type1 ? t1 : 24 + 3 * t2) + 16 * wc;
        K1_QKV_ADDR(col0, 4 * fq, voff, step, sc);
        const bool isK = step == 256u;
#pragma unroll
        for (int m = 0; m < 8; ++m) {
            const f32x4 v = acc[m][4] * sc;
            u32x2 w; w.x = cvtpk(v[0], v[1]); w.y = cvtpk(v[2], v[3]);
            __builtin_amdgcn_raw_buffer_store_b64(w, rs, voff + (isK ? K1_ROWOFF(true, m) : K1_ROWOFF(false, m)), 0, K1_ST_AUX);
        }
    }
    if (roleP) {
        const int hcol = (type1 ? 128 * t1 : 3072 + 64 * t2) + 32 * wc + 8 * fq;
        const unsigned voff = (unsigned)WS_A + (unsigned)(row0 * (KTOT * 2) + hcol * 2);
#pragma unroll
        for (int m = 0; m < 8; ++m) {
            const f32x2 h00 = geglu2((f32x2){acc[m][0][0], acc[m][0][1]}, (f32x2){acc[m][2][0], acc[m][2][1]}), h01 = geglu2((f32x2){acc[m][0][2], acc[m][0][3]}, (f32x2){acc[m][2][2], acc[m][2][3]});
            const f32x2 h10 = geglu2((f32x2){acc[m][1][0], acc[m][1][1]}, (f32x2){acc[m][3][0], acc[m][3][1]}), h11 = geglu2((f32x2){acc[m][1][2], acc[m][1][3]}, (f32x2){acc[m][3][2], acc[m][3][3]});
            const unsigned a0 = cvtpk(h00.x, h00.y), a1 = cvtpk(h01.x, h01.y), b0 = cvtpk(h10.x, h10.y), b1 = cvtpk(h11.x, h11.y);
            u32x4 w; w.x = odd ? b0 : a0; w.y = odd ? b1 : a1; w.z = odd ? a0 : b0; w.w = odd ? a1 : b1;
            __builtin_amdgcn_raw_buffer_store_b128(w, rs, voff + (unsigned)(m * 16 * KTOT * 2), 0, K1_ST_AUX);
        }
    } else {
#pragma unroll
        for (int pr = 0; pr < 2; ++pr) {
            const int col0 = 64 * (24 + 3 * t2 + (wc - 1)) + 32 * pr;
            K1_QKV_ADDR(col0, 8 * fq, voff, step, sc);
            const bool isK = step == 256u;
#pragma unroll
            for (int m = 0; m < 8; ++m) {
                const f32x4 v0 = acc[m][2 * pr] * sc, v1 = acc[m][2 * pr + 1] * sc;
                const unsigned a0 = cvtpk(v0[0], v0[1]), a1 = cvtpk(v0[2], v0[3]), b0 = cvtpk(v1[0], v1[1]), b1 = cvtpk(v1[2], v1[3]);
                u32x4 w; w.x = odd ? b0 : a0; w.y = odd ? b1 : a1; w.z = odd ? a0 : b0; w.w = odd ? a1 : b1;
                __builtin_amdgcn_raw_buffer_store_b128(w, rs, voff + (isK ? K1_ROWOFF(true, m) : K1_ROWOFF(false, m)), 0, K1_ST_AUX);
            }
        }
    }
#undef K1_QKV_ADDR
#undef K1_ROWOFF
    }
    if (tid == 0) ((unsigned*)(ws + WS_FLAG))[bid * 64] = 0u;
    if (tid == 64 && bid < 32) ((unsigned*)(ws + WS_FLAG))[(256 + bid) * 64] = 0u;
    if (tid >= 128 && tid < 136) ((unsigned*)(ws + WS_XC))[(bid * 8 + (tid - 128)) * 32] = 0u;
    STAMP(0, 3);
    WAIT_V(0);
    STAMP(0, 4);
#undef K1_SEG
#undef K1_DMA_A
#undef K1_DMA_B
}

#define OSC1 1
constexpr int K3_STG = 49152;
constexpr int K23_SCR = 3 * K3_STG;
constexpr int K23_LDS = K23_SCR + 1024;
#define K2_RING(j) (K3_STG + (((j) + 1) % 3) * 32768)
__device__ __forceinline__ int crow(int r, int hi) { return (r & 3) + 8 * (r >> 2) + 4 * hi; }
#define MFMA32(a, b, c) __builtin_amdgcn_mfma_f32_32x32x16_bf16(a, b, c, 0, 0, 0)
#define MFMA16(a, b, c) __builtin_amdgcn_mfma_f32_16x16x32_bf16(a, b, c, 0, 0, 0)
#define MX3(a, b, c) __builtin_fmaxf(__builtin_fmaxf((a), (b)), (c))
#define PIN(x) asm volatile("" : "+v"(x))
#define EX2(v) __builtin_amdgcn_exp2f(v)
__device__ __forceinline__ void glds4_sc1(const char* sbase, unsigned voff, unsigned ldsaddr) {
    asm volatile("s_mov_b32 m0, %2\n\ts_nop 0\n\tglobal_load_lds_dword %0, %1 sc1" :: "v"(voff), "s"(sbase), "s"(ldsaddr) : "memory"); }
__device__ __forceinline__ void glds4(const char* sbase, unsigned voff, unsigned ldsaddr) {
    asm volatile("s_mov_b32 m0, %2\n\ts_nop 0\n\tglobal_load_lds_dword %0, %1" :: "v"(voff), "s"(sbase), "s"(ldsaddr) : "memory"); }

__global__ __launch_bounds__(768, 3) void k23(const bf16_t* __restrict__ wo, const bf16_t* __restrict__ wd, unsigned char* __restrict__ ws, bf16_t* __restrict__ out) {
    extern __shared__ __attribute__((aligned(16))) char smem[];
    const unsigned lds = (unsigned)(uintptr_t)(LAS char*)smem;
    const int tid = threadIdx.x, wid = __builtin_amdgcn_readfirstlane(tid >> 6), lane = tid & 63;
    const int bid = blockIdx.x, xcd = bid & 7, idx = bid >> 3;
    unsigned* flags = (unsigned*)(ws + WS_FLAG);
    const int kq = xcd >> 1, mb = (xcd & 1) * 4 + (idx >> 3), nb = idx & 7;
    const int head = xcd * 4 + (idx & 3), qb = idx >> 2;
    LAS unsigned* fl = (LAS unsigned*)(uintptr_t)(lds + K23_SCR);
    CODE_LABEL("gm_k23_beg");

    if (wid >= 8) {
        const int L = wid - 8;
        const int lr = lane >> 3, ls = lane & 7;
        const char* Kg = (const char*)(ws + WS_K) + (size_t)head * 131072;
        const char* Vg = (const char*)(ws + WS_V) + (size_t)head * 131072;
        const unsigned voff = (unsigned)(L * 4096 + lane * 16);
#define K2_DMA(j) do { const char* _k = Kg + (size_t)(j) * 16384; const char* _v = Vg + (size_t)(j) * 16384; const unsigned _d = lds + K2_RING(j) + L * 4096; \
        _Pragma("unroll") for (int _i = 0; _i < 4; ++_i) glds16(_k + _i * 1024, voff, _d + _i * 1024); \
        _Pragma("unroll") for (int _i = 0; _i < 4; ++_i) glds16(_v + _i * 1024, voff, _d + 16384 + _i * 1024); } while (0)
        const unsigned voffA0 = (unsigned)(lr * (KTOT * 2) + ((ls ^ ((lane >> 4) & 7)) * 16)), voffA1 = (unsigned)(lr * (KTOT * 2) + ((ls ^ ((4 + (lane >> 4)) & 7)) * 16));
        const unsigned voffB0 = (unsigned)(lr * (DM * 2) + ((ls ^ (3 * ((lane >> 4) & 1))) * 16)), voffB1 = (unsigned)(lr * (DM * 2) + ((ls ^ (3 * ((lane >> 4) & 1) + 4)) * 16));
        const char* Ag = (const char*)(ws + WS_A) + (size_t)(mb * 256 + L * 32) * KTOT * 2;
#define K3_DMA_A(tt, h) do { const int _t = (tt) < 18 ? (tt) : 17; const unsigned _d = lds + ((tt) % 3) * K3_STG + (h) * 16384 + L * 4096; \
        const char* _s = Ag + (_t < 14 ? (14 * kq + _t) * 128 : FF * 2 + (4 * kq + _t - 14) * 128) + (size_t)((h) * 128) * KTOT * 2; \
        if (!OSC1 || _t < 14) { glds16(_s, voffA0, _d); glds16(_s + (size_t)8 * KTOT * 2, voffA1, _d + 1024); glds16(_s + (size_t)16 * KTOT * 2, voffA0, _d + 2048); glds16(_s + (size_t)24 * KTOT * 2, voffA1, _d + 3072); } \
        else { glds16_sc1(_s, voffA0, _d); glds16_sc1(_s + (size_t)8 * KTOT * 2, voffA1, _d + 1024); glds16_sc1(_s + (size_t)16 * KTOT * 2, voffA0, _d + 2048); glds16_sc1(_s + (size_t)24 * KTOT * 2, voffA1, _d + 3072); } } while (0)
#define K3_DMA_B(tt, g) do { const int _t = (tt) < 18 ? (tt) : 17; const unsigned _d = lds + ((tt) % 3) * K3_STG + 32768 + (g) * 8192 + L * 2048; \
        const char* _w = (_t < 14 ? (const char*)wd + (size_t)(14 * kq + _t) * 64 * DM * 2 : (const char*)wo + (size_t)(4 * kq + _t - 14) * 64 * DM * 2) + nb * 256 + (g) * 128 + (size_t)(L * 16) * DM * 2; \
        glds16(_w, voffB0, _d); glds16(_w + (size_t)8 * DM * 2, voffB1, _d + 1024); } while (0)
        K2_DMA(0); K2_DMA(1);
        {
            const char* cb; int cl;
            CODE_RANGE(cb, cl, "gm_k23_ref", "gm_k23_beg", "gm_k23_end");
            const int off = ((L & 1) * 64 + lane) * 128;
            if (L >= 2) CODE_RANGE(cb, cl, "gm_k23_ref2", "gm_k23_ref2", "gm_k23_end2");
            if (cl > 0) glds4(cb, (unsigned)(off < cl - 4 ? off : 0), lds + K23_SCR + 256);
        }
        WAIT_V(0); BAR();
#pragma unroll
        for (int j = 0; j < 8; ++j) {
            if (j == 1 || j == 2) { WAIT_V(0); BAR(); } else if (j >= 3 && j <= 5) { WAIT_V(1); BAR(); } else if (j == 6) { WAIT_V(13); BAR(); } else if (j == 7) { BAR(); }
            if (j + 2 < 8) K2_DMA(j + 2);
            if (j >= 2 && j <= 5) {
                const int ins = (j - 2) * 4 + L;
                if (ins < 9) {
                    const int row0 = (idx * 9 + ins) * 4;
                    const char* base = row0 < 896 ? (const char*)wd + (size_t)(896 * kq + row0) * 2048 : (const char*)wo + (size_t)(256 * kq + row0 - 896) * 2048;
                    glds4(base, (unsigned)(lane * 128), lds + K23_SCR + 256);
                } else {
                    const int e = ins < 14 ? ins - 9 : ins - 11;
                    const int x = e * 32 + idx;
                    const char* base = (const char*)(ws + WS_A) + (size_t)((xcd & 1) * 1024 + (x & 15) * 64) * (KTOT * 2) + (14 * kq + (x >> 4)) * 128;
                    glds4(base, (unsigned)(lane * (KTOT * 2)), lds + K23_SCR + 256);
                }
            }
            if (j == 5) { K3_DMA_A(0, 0); K3_DMA_B(0, 0); K3_DMA_B(0, 1); K3_DMA_A(0, 1); }
            if (j == 6) { K3_DMA_A(1, 0); K3_DMA_A(1, 1); }
            if (j == 7) { K3_DMA_B(1, 0); K3_DMA_B(1, 1); }
        }
        WAIT_V(0); BAR();
        const unsigned redo = __builtin_amdgcn_readfirstlane(__any(fl[lane & 7] != 0u) ? 1 : 0);
        if (redo) {
            K2_DMA(0); K2_DMA(1);
            for (int j = 0; j < 8; ++j) {
                if (j < 7) WAIT_V(8); else WAIT_V(0);
                BAR();
                if (j + 2 < 8) K2_DMA(j + 2);
            }
            BAR();
            K3_DMA_A(1, 0); K3_DMA_B(1, 0); K3_DMA_B(1, 1); K3_DMA_A(1, 1);
            WAIT_V(0); BAR();
        }
#define K3_DMA_T(tt) do { K3_DMA_A(tt, 0); K3_DMA_B(tt, 0); K3_DMA_B(tt, 1); K3_DMA_A(tt, 1); } while (0)
        K3_DMA_T(2);
        int ost = 0;
        for (int t = 0; t < 18; ++t) {
            if (L == 0 && t >= 4 && t <= 11) {
                if (t == 8 || t == 10 || t == 11) {
                    WAIT_V(12);
                    if (ost == 0) {
                        if ((unsigned)__builtin_amdgcn_readfirstlane(fl[128]) >= 8u) ost = 2;
                        else if (t == 11) {
                            unsigned spins = 0;
                            while (__builtin_amdgcn_readfirstlane(__hip_atomic_load(flags + (256 + mb * 4 + kq) * 64, RLX_AGENT)) < 8u) { __builtin_amdgcn_s_sleep(1); if (++spins > (1u << 22)) break; }
                            ost = 2;
                        }
                        if (OSC1 && ost == 2) ost = 1;
                        if (ost == 2 && t == 11) { __builtin_amdgcn_fence(__ATOMIC_ACQUIRE, "agent"); WAIT_V(0); ost = 1; }
                    } else if (t == 11 && ost == 3) WAIT_V(0);
                } else WAIT_V(13);
            } else if (t < 16) WAIT_V(12);
            else WAIT_V(0);
            BAR();
            if (t + 3 < 18) K3_DMA_T(t + 3);
            if (L == 0) {
                if (t == 3) { if (lane == 0) __hip_atomic_fetch_add(flags + (256 + qb * 4 + (xcd >> 1)) * 64, 1u, RLX_AGENT); }
                else if (t == 5 || (t == 8 && ost == 0)) glds4_sc1((const char*)(flags + (256 + mb * 4 + kq) * 64), 0u, lds + K23_SCR + 512);
                else if (ost == 2) { asm volatile("buffer_inv sc1" ::: "memory"); ost = (t == 10) ? 3 : 1; }
            }
        }
#undef K3_DMA_T
        CODE_LABEL("gm_k23_end2");
        return;
#undef K2_DMA
#undef K3_DMA_A
#undef K3_DMA_B
    }

    STAMP(1, 0);
    float omg[4];
    { const float* gv = (const float*)(ws + WS_G) + mb * 256 + (wid >> 2) * 128 + ((wid & 3) >> 1) * 64 + (lane & 15);
#pragma unroll
      for (int m = 0; m < 4; ++m) omg[m] = 1.0f - gv[16 * m]; }

    {
        const int r32 = lane & 31, hi = lane >> 5;
        const bf16_t* Qg = (const bf16_t*)(ws + WS_Q) + ((size_t)head * SEQ + qb * 256 + wid * 32) * 32;
        bf16x8 qf0 = *(const bf16x8*)(Qg + r32 * 32 + hi * 8);
        bf16x8 qf1 = *(const bf16x8*)(Qg + r32 * 32 + 16 + hi * 8);
        float gval = ((const float*)(ws + WS_G))[qb * 256 + wid * 32 + r32];
        bf16x8 ones;
        { const short o1 = (((lane >> 4) & 1) == (((lane & 15) >> 2) & 1)) ? (short)0x3F80 : (short)0;
#pragma unroll
          for (int j = 0; j < 8; ++j) ones[j] = o1; }
        const unsigned koff = lds + hi * 1024 + r32 * 16;
        const unsigned voffr = lds + 16384 + ((lane >> 4) & 1) * 32 + (lane & 3) * 8 + (4 * hi + ((lane & 15) >> 2)) * 64;
        const unsigned koff0 = koff + K2_RING(0);
        f32x16 o = {}; f32x4 lsum = {0.f, 0.f, 0.f, 0.f};
        asm volatile("" : "+v"(omg[0]), "+v"(omg[1]), "+v"(omg[2]), "+v"(omg[3]), "+v"(gval), "+v"(qf0), "+v"(qf1));
        BAR();
        STAMP(1, 1);
        {
            f32x16 SA, SB, negm;
            u32x4 WA0, WA1, WB0 = {0u, 0u, 0u, 0u}, WB1 = {0u, 0u, 0u, 0u};
            bf16x8 kf0, kf1, vf0 = {}, vf1 = {};
            kf0 = *(LAS const bf16x8*)(LDSP(koff0)); kf1 = *(LAS const bf16x8*)(LDSP(koff0) + 2048);
            { f32x16 z = {}; SA = MFMA32(kf0, qf0, z); SA = MFMA32(kf1, qf1, SA); }
            kf0 = *(LAS const bf16x8*)(LDSP(koff0) + 512); kf1 = *(LAS const bf16x8*)(LDSP(koff0) + 2048 + 512);
            {
                float a = MX3(SA[0], SA[1], SA[2]), b = MX3(SA[3], SA[4], SA[5]);
                a = MX3(a, SA[6], SA[7]); b = MX3(b, SA[8], SA[9]); a = MX3(a, SA[10], SA[11]); b = MX3(b, SA[12], SA[13]); a = MX3(a, SA[14], SA[15]);
                float rm = __builtin_fmaxf(a, b);
                auto rr = __builtin_amdgcn_permlane32_swap(__float_as_uint(rm), __float_as_uint(rm), false, false); rm = __builtin_fmaxf(__uint_as_float(rr[0]), __uint_as_float(rr[1]));
#pragma unroll
                for (int r = 0; r < 16; ++r) { SA[r] -= rm; negm[r] = -rm; }
                PIN(negm);
            }
#define K2_STEP(C, N, WP0, WP1, WC0, WC1, KADDR, VADDR) do { \
            N = MFMA32(kf0, qf0, negm); \
            C[0] = EX2(C[0]); C[1] = EX2(C[1]); C[2] = EX2(C[2]); C[3] = EX2(C[3]); PIN(C); SCHED(); \
            kf0 = *(LAS const bf16x8*)(KADDR); SCHED(); \
            o = MFMA32(vf0, __builtin_bit_cast(bf16x8, WP0), o); \
            C[4] = EX2(C[4]); C[5] = EX2(C[5]); C[6] = EX2(C[6]); C[7] = EX2(C[7]); PIN(C); SCHED(); \
            vf0 = cat8(trread(VADDR), trread((VADDR) + 512)); SCHED(); \
            lsum = MFMA16(ones, __builtin_bit_cast(bf16x8, WP0), lsum); \
            WC0[0] = cvtpk(C[0], C[1]); WC0[1] = cvtpk(C[2], C[3]); WC0[2] = cvtpk(C[4], C[5]); WC0[3] = cvtpk(C[6], C[7]); PIN(WC0); SCHED(); \
            N = MFMA32(kf1, qf1, N); \
            C[8] = EX2(C[8]); C[9] = EX2(C[9]); C[10] = EX2(C[10]); C[11] = EX2(C[11]); PIN(C); SCHED(); \
            kf1 = *(LAS const bf16x8*)((KADDR) + 2048); SCHED(); \
            o = MFMA32(vf1, __builtin_bit_cast(bf16x8, WP1), o); \
            C[12] = EX2(C[12]); C[13] = EX2(C[13]); C[14] = EX2(C[14]); C[15] = EX2(C[15]); PIN(C); SCHED(); \
            vf1 = cat8(trread((VADDR) + 1024), trread((VADDR) + 1536)); SCHED(); \
            lsum = MFMA16(ones, __builtin_bit_cast(bf16x8, WP1), lsum); \
            WC1[0] = cvtpk(C[8], C[9]); WC1[1] = cvtpk(C[10], C[11]); WC1[2] = cvtpk(C[12], C[13]); WC1[3] = cvtpk(C[14], C[15]); PIN(WC1); SCHED(); \
            } while (0)
            for (int j = 0; j < 8; ++j) {
                if (j > 0) { WAIT_L(0); BAR(); }
                LAS const char* kc = LDSP(koff + K2_RING(j));
                LAS const char* kn = LDSP(koff + K2_RING(j + 1));
                LAS const char* vc = LDSP(voffr + K2_RING(j));
                K2_STEP(SA, SB, WB0, WB1, WA0, WA1, kc + 1 * 4096, vc + 0 * 2048);
                K2_STEP(SB, SA, WA0, WA1, WB0, WB1, kc + 1 * 4096 + 512, vc + 1 * 2048);
                K2_STEP(SA, SB, WB0, WB1, WA0, WA1, kc + 2 * 4096, vc + 2 * 2048);
                K2_STEP(SB, SA, WA0, WA1, WB0, WB1, kc + 2 * 4096 + 512, vc + 3 * 2048);
                K2_STEP(SA, SB, WB0, WB1, WA0, WA1, kc + 3 * 4096, vc + 4 * 2048);
                K2_STEP(SB, SA, WA0, WA1, WB0, WB1, kc + 3 * 4096 + 512, vc + 5 * 2048);
                K2_STEP(SA, SB, WB0, WB1, WA0, WA1, kn, vc + 6 * 2048);
                K2_STEP(SB, SA, WA0, WA1, WB0, WB1, kn + 512, vc + 7 * 2048);
            }
            o = MFMA32(vf0, __builtin_bit_cast(bf16x8, WB0), o); lsum = MFMA16(ones, __builtin_bit_cast(bf16x8, WB0), lsum);
            o = MFMA32(vf1, __builtin_bit_cast(bf16x8, WB1), o); lsum = MFMA16(ones, __builtin_bit_cast(bf16x8, WB1), lsum);
#undef K2_STEP
        }
        STAMP(1, 2);
        const __amdgpu_buffer_rsrc_t rso = __builtin_amdgcn_make_buffer_rsrc((void*)(ws + WS_A), (short)0, (int)(SEQ * KTOT * 2), 0x00020000);
        const unsigned ob = (unsigned)(((qb * 256 + wid * 32 + r32) * KTOT + FF + head * 32 + 8 * hi) * 2);
#define K2_STORE_O() do { \
            const float sc = gval * __builtin_amdgcn_rcpf(lsum[0]); \
            unsigned w[8]; \
            _Pragma("unroll") for (int g = 0; g < 4; ++g) { w[2 * g] = cvtpk(o[4 * g] * sc, o[4 * g + 1] * sc); w[2 * g + 1] = cvtpk(o[4 * g + 2] * sc, o[4 * g + 3] * sc); } \
            _Pragma("unroll") for (int pr = 0; pr < 2; ++pr) { \
                auto r0 = __builtin_amdgcn_permlane32_swap(w[4 * pr], w[4 * pr + 2], false, false); \
                auto r1 = __builtin_amdgcn_permlane32_swap(w[4 * pr + 1], w[4 * pr + 3], false, false); \
                const u32x4 v = {r0[0], r1[0], r0[1], r1[1]}; \
                __builtin_amdgcn_raw_buffer_store_b128(v, rso, ob + pr * 32, 0, 16); } } while (0)
        {
            const bool bad = !(lsum[0] <= 1.0e9f);
            const unsigned badw = __any(bad) ? 1u : 0u;
            if (lane == 0) fl[wid] = badw;
            K2_STORE_O();
            STAMP(1, 3);
            WAIT_L(0); BAR();
            const unsigned redo = __builtin_amdgcn_readfirstlane(__any(fl[lane & 7] != 0u) ? 1 : 0);
            if (redo) {
                o = (f32x16){}; lsum = (f32x4){0.f, 0.f, 0.f, 0.f};
                float mhat = 0.f; f32x16 negm = {};
                bool first = true;
                for (int j = 0; j < 8; ++j) {
                    WAIT_L(0); BAR();
                    LAS const char* kb = LDSP(koff + K2_RING(j));
                    LAS const char* vb = LDSP(voffr + K2_RING(j));
#pragma unroll 1
                    for (int tt = 0; tt < 4; ++tt) {
                        LAS const char* kp = kb + tt * 4096;
                        LAS const char* vp = vb + tt * 4096;
                        const bf16x8 k00 = *(LAS const bf16x8*)(kp), k01 = *(LAS const bf16x8*)(kp + 2048), k10 = *(LAS const bf16x8*)(kp + 512), k11 = *(LAS const bf16x8*)(kp + 2048 + 512);
                        f32x16 p0 = MFMA32(k00, qf0, negm); p0 = MFMA32(k01, qf1, p0);
                        f32x16 p1 = MFMA32(k10, qf0, negm); p1 = MFMA32(k11, qf1, p1);
                        float a = MX3(p0[0], p0[1], p1[0]), b = MX3(p0[2], p0[3], p1[1]); a = MX3(a, p1[2], p1[3]);
#pragma unroll
                        for (int r = 4; r < 16; r += 4) { a = MX3(a, p0[r], p0[r + 1]); b = MX3(b, p0[r + 2], p0[r + 3]); a = MX3(a, p1[r], p1[r + 1]); b = MX3(b, p1[r + 2], p1[r + 3]); }
                        float rm = __builtin_fmaxf(a, b);
                        { auto rr = __builtin_amdgcn_permlane32_swap(__float_as_uint(rm), __float_as_uint(rm), false, false); rm = __builtin_fmaxf(__uint_as_float(rr[0]), __uint_as_float(rr[1])); }
                        {
                            const float dl = first ? rm : __builtin_fmaxf(rm, 0.f);
                            mhat += dl;
#pragma unroll
                            for (int r = 0; r < 16; ++r) { p0[r] -= dl; p1[r] -= dl; negm[r] = -mhat; }
                            if (!first) { const float f = EX2(-dl); o = o * f; lsum = lsum * f; }
                            first = false;
                        }
#pragma unroll
                        for (int r = 0; r < 16; ++r) { p0[r] = EX2(p0[r]); p1[r] = EX2(p1[r]); }
                        const u32x4 w0 = {cvtpk(p0[0], p0[1]), cvtpk(p0[2], p0[3]), cvtpk(p0[4], p0[5]), cvtpk(p0[6], p0[7])};
                        const u32x4 w1 = {cvtpk(p0[8], p0[9]), cvtpk(p0[10], p0[11]), cvtpk(p0[12], p0[13]), cvtpk(p0[14], p0[15])};
                        const u32x4 w2 = {cvtpk(p1[0], p1[1]), cvtpk(p1[2], p1[3]), cvtpk(p1[4], p1[5]), cvtpk(p1[6], p1[7])};
                        const u32x4 w3 = {cvtpk(p1[8], p1[9]), cvtpk(p1[10], p1[11]), cvtpk(p1[12], p1[13]), cvtpk(p1[14], p1[15])};
                        const bf16x8 v0 = cat8(trread(vp), trread(vp + 512)), v1 = cat8(trread(vp + 1024), trread(vp + 1536));
                        const bf16x8 v2 = cat8(trread(vp + 2048), trread(vp + 2560)), v3 = cat8(trread(vp + 3072), trread(vp + 3584));
                        const bf16x8 f0 = __builtin_bit_cast(bf16x8, w0), f1 = __builtin_bit_cast(bf16x8, w1), f2 = __builtin_bit_cast(bf16x8, w2), f3 = __builtin_bit_cast(bf16x8, w3);
                        o = MFMA32(v0, f0, o); lsum = MFMA16(ones, f0, lsum);
                        o = MFMA32(v1, f1, o); lsum = MFMA16(ones, f1, lsum);
                        o = MFMA32(v2, f2, o); lsum = MFMA16(ones, f2, lsum);
                        o = MFMA32(v3, f3, o); lsum = MFMA16(ones, f3, lsum);
                    }
                }
                WAIT_L(0); BAR();
                WAIT_V(0);
                K2_STORE_O();
                BAR();
            }
        }
        STAMP(1, 4);
#undef K2_STORE_O
    }

    {
        const int G = wid >> 2, j4 = wid & 3, wm = j4 >> 1, wn = j4 & 1, fr = lane & 15, fq = lane >> 4;
        const int arow = G * 128 + wm * 64 + fr;
        const unsigned aoff0 = lds + arow * 128 + ((fq ^ (fr >> 1)) * 16);
        const unsigned aoff1 = lds + arow * 128 + (((fq ^ (fr >> 1)) ^ 4) * 16);
        const int q4 = (lane >> 2) & 3, p4 = lane & 3;
        const int sR = 3 * ((q4 >> 1) & 1) + 4 * (fq & 1);
        const int krow = (8 * fq + q4) * 128;
        const unsigned boff00 = lds + 32768 + wn * 8192 + krow + ((p4 ^ sR) * 16) + (p4 & 1) * 8;
        const unsigned boff01 = lds + 32768 + wn * 8192 + krow + ((p4 ^ sR) * 16) + ((p4 & 1) ^ 1) * 8;
        const unsigned boff10 = lds + 32768 + wn * 8192 + krow + (((4 + p4) ^ sR) * 16) + (p4 & 1) * 8;
        const unsigned boff11 = lds + 32768 + wn * 8192 + krow + (((4 + p4) ^ sR) * 16) + ((p4 & 1) ^ 1) * 8;
        f32x4 acc[4][4];
#pragma unroll
        for (int m = 0; m < 4; ++m)
#pragma unroll
            for (int n = 0; n < 4; ++n) acc[m][n] = (f32x4){0.f, 0.f, 0.f, 0.f};
        unsigned fts = 0;
        STAMP(2, 0);
        STAMP(2, 1);
#define K3_READ(F_A, F_B, so_, ks_) do { \
            F_B[0] = cat8(trread(LDSP(boff00 + (so_)) + (ks_) * 4096), trread(LDSP(boff00 + (so_)) + (ks_) * 4096 + 512)); \
            F_A[0] = *(LAS const bf16x8*)(LDSP(((ks_) ? aoff1 : aoff0) + (so_))); \
            F_B[1] = cat8(trread(LDSP(boff01 + (so_)) + (ks_) * 4096), trread(LDSP(boff01 + (so_)) + (ks_) * 4096 + 512)); \
            F_A[1] = *(LAS const bf16x8*)(LDSP(((ks_) ? aoff1 : aoff0) + (so_)) + 2048); \
            F_B[2] = cat8(trread(LDSP(boff10 + (so_)) + (ks_) * 4096), trread(LDSP(boff10 + (so_)) + (ks_) * 4096 + 512)); \
            F_A[2] = *(LAS const bf16x8*)(LDSP(((ks_) ? aoff1 : aoff0) + (so_)) + 4096); \
            F_B[3] = cat8(trread(LDSP(boff11 + (so_)) + (ks_) * 4096), trread(LDSP(boff11 + (so_)) + (ks_) * 4096 + 512)); \
            F_A[3] = *(LAS const bf16x8*)(LDSP(((ks_) ? aoff1 : aoff0) + (so_)) + 6144); } while (0)
#define K3_RB(dst, boff, so_, ks_) dst = cat8(trread(LDSP((boff) + (so_)) + (ks_) * 4096), trread(LDSP((boff) + (so_)) + (ks_) * 4096 + 512))
#define K3_RA(dst, so_, ks_, m_) dst = *(LAS const bf16x8*)(LDSP(((ks_) ? aoff1 : aoff0) + (so_)) + (m_) * 2048)
#define K3_M(CA, CB, m_, n_) acc[m_][n_] = MFMA16(CB[n_], CA[m_], acc[m_][n_])
#define K3_STEP(CA, CB, NA, NB, so_, ks_) do { \
            K3_M(CA, CB, 0, 0); K3_RA(NA[0], so_, ks_, 0); SCHED(); \
            K3_M(CA, CB, 0, 1); K3_RB(NB[0], boff00, so_, ks_); SCHED(); \
            K3_M(CA, CB, 0, 2); SCHED(); \
            K3_M(CA, CB, 0, 3); K3_RB(NB[1], boff01, so_, ks_); SCHED(); \
            K3_M(CA, CB, 1, 0); SCHED(); \
            K3_M(CA, CB, 1, 1); K3_RB(NB[2], boff10, so_, ks_); SCHED(); \
            K3_M(CA, CB, 1, 2); SCHED(); \
            K3_M(CA, CB, 1, 3); K3_RB(NB[3], boff11, so_, ks_); SCHED(); \
            K3_M(CA, CB, 2, 0); SCHED(); \
            K3_M(CA, CB, 2, 1); K3_RA(NA[1], so_, ks_, 1); SCHED(); \
            K3_M(CA, CB, 2, 2); K3_RA(NA[2], so_, ks_, 2); SCHED(); \
            K3_M(CA, CB, 2, 3); K3_RA(NA[3], so_, ks_, 3); SCHED(); \
            K3_M(CA, CB, 3, 0); K3_M(CA, CB, 3, 1); K3_M(CA, CB, 3, 2); K3_M(CA, CB, 3, 3); SCHED(); } while (0)
        bf16x8 a0[4], b0[4], a1[4], b1[4];
        K3_READ(a0, b0, 0u, 0);
        for (int t = 0; t < 18; ++t) {
            const unsigned so = (t % 3) * K3_STG, sn = ((t + 1) % 3) * K3_STG;
            if (t == 14) {
#pragma unroll
                for (int m = 0; m < 4; ++m)
#pragma unroll
                    for (int n = 0; n < 4; ++n) acc[m][n] = acc[m][n] * omg[m];
            }
            SCHED();
            K3_STEP(a0, b0, a1, b1, so, 1);
            if (t == 3) WAIT_V(0);
            WAIT_L(0); SCHED(); BAR(); SCHED();
            K3_STEP(a1, b1, a0, b0, sn, 0);
        }
#undef K3_STEP
#undef K3_M
#undef K3_RA
#undef K3_RB
#undef K3_READ
        STAMP(2, 2);
        {
            const __amdgpu_buffer_rsrc_t rs = __builtin_amdgcn_make_buffer_rsrc((void*)(ws + WS_P), (short)0, (int)(256 * 65536), 0x00020000);
            const unsigned so = (unsigned)(bid * 65536 + wid * 2048 + lane * 16);
            f32x4 fin[4];
#define K3_SEND(q) do { \
                const u32x4 w0 = {cvtpk(acc[q][0][0], acc[q][0][1]), cvtpk(acc[q][0][2], acc[q][0][3]), cvtpk(acc[q][1][0], acc[q][1][1]), cvtpk(acc[q][1][2], acc[q][1][3])}; \
                const u32x4 w1 = {cvtpk(acc[q][2][0], acc[q][2][1]), cvtpk(acc[q][2][2], acc[q][2][3]), cvtpk(acc[q][3][0], acc[q][3][1]), cvtpk(acc[q][3][2], acc[q][3][3])}; \
                __builtin_amdgcn_raw_buffer_store_b128(w0, rs, so + (q) * 16384, 0, 16); __builtin_amdgcn_raw_buffer_store_b128(w1, rs, so + (q) * 16384 + 1024, 0, 16); } while (0)
#define K3_KEEP(q) do { _Pragma("unroll") for (int n = 0; n < 4; ++n) fin[n] = acc[q][n]; } while (0)
            if (kq == 0) { K3_SEND(1); K3_SEND(2); K3_SEND(3); K3_KEEP(0); }
            else if (kq == 1) { K3_SEND(0); K3_SEND(2); K3_SEND(3); K3_KEEP(1); }
            else if (kq == 2) { K3_SEND(0); K3_SEND(1); K3_SEND(3); K3_KEEP(2); }
            else { K3_SEND(0); K3_SEND(1); K3_SEND(2); K3_KEEP(3); }
#undef K3_SEND
#undef K3_KEEP
            STAMP(2, 3);
            WAIT_V(0);
            STAMP(2, 4);
            unsigned* xcnt = (unsigned*)(ws + WS_XC);
            if (lane < 3) { const int q = (kq + 1 + lane) & 3; __hip_atomic_fetch_add(xcnt + ((idx * 8 + 2 * q + (xcd & 1)) * 8 + wid) * 32, 1u, RLX_AGENT); }
            {
                unsigned spins = 0;
                while (__builtin_amdgcn_readfirstlane(__hip_atomic_load(xcnt + (bid * 8 + wid) * 32, RLX_AGENT)) < 3u) { __builtin_amdgcn_s_sleep(1); if (++spins > (1u << 22)) break; }
            }
            asm volatile("" ::: "memory");
            STAMP(2, 5);
            u32x4 rv[3][2];
#pragma unroll
            for (int i = 0; i < 3; ++i) {
                const int q = (kq + 1 + i) & 3;
                const unsigned po = (unsigned)((idx * 8 + 2 * q + (xcd & 1)) * 65536 + kq * 16384 + wid * 2048 + lane * 16);
                rv[i][0] = __builtin_amdgcn_raw_buffer_load_b128(rs, po, 0, 16); rv[i][1] = __builtin_amdgcn_raw_buffer_load_b128(rs, po + 1024, 0, 16);
            }
#pragma unroll
            for (int i = 0; i < 3; ++i)
#pragma unroll
                for (int n = 0; n < 4; ++n) {
                    const unsigned u0 = rv[i][n >> 1][(n & 1) * 2], u1 = rv[i][n >> 1][(n & 1) * 2 + 1];
                    fin[n][0] += __uint_as_float(u0 << 16); fin[n][1] += __uint_as_float(u0 & 0xffff0000u);
                    fin[n][2] += __uint_as_float(u1 << 16); fin[n][3] += __uint_as_float(u1 & 0xffff0000u);
                }
            const int row = mb * 256 + G * 128 + wm * 64 + kq * 16 + fr;
            const bool odd = fq & 1;
#pragma unroll
            for (int pr = 0; pr < 2; ++pr) {
                const f32x4 v0 = fin[2 * pr], v1 = fin[2 * pr + 1];
                const unsigned a0 = cvtpk(v0[0], v0[1]), a1 = cvtpk(v0[2], v0[3]), b0 = cvtpk(v1[0], v1[1]), b1 = cvtpk(v1[2], v1[3]);
                u32x4 w; w.x = odd ? b0 : a0; w.y = odd ? b1 : a1; w.z = odd ? a0 : b0; w.w = odd ? a1 : b1;
                *(u32x4*)(out + (size_t)row * DM + nb * 128 + wn * 64 + pr * 32 + 8 * fq) = w;
            }
        }
        STAMP(2, 6);
        WAIT_V(0);
        STAMP(2, 7);
    }
    CODE_LABEL("gm_k23_end");
}

extern "C" void kernel_launch(void* const* d_in, const int* in_sizes, int n_in, void* d_out, int out_size, void* d_ws, size_t ws_size, hipStream_t stream) {
    const bf16_t* x = (const bf16_t*)d_in[0];
    const bf16_t* wqkv = (const bf16_t*)d_in[1];
    const bf16_t* wo = (const bf16_t*)d_in[2];
    const bf16_t* wg = (const bf16_t*)d_in[4];
    const bf16_t* wu = (const bf16_t*)d_in[5];
    const bf16_t* wd = (const bf16_t*)d_in[6];
    const bf16_t* gw = (const bf16_t*)d_in[7];
    unsigned char* ws = (unsigned char*)d_ws;
    (void)hipFuncSetAttribute((const void*)k1_gemm, hipFuncAttributeMaxDynamicSharedMemorySize, K1_LDS);
    (void)hipFuncSetAttribute((const void*)k23, hipFuncAttributeMaxDynamicSharedMemorySize, K23_LDS);
    k1_gemm<<<256, 512, K1_LDS, stream>>>(x, wqkv, wg, wu, gw, ws);
    k23<<<256, 768, K23_LDS, stream>>>(wo, wd, ws, (bf16_t*)d_out);
}
```

```cpp
#include <hip/hip_runtime.h>
#include <hip/hip_bf16.h>
#include <stdint.h>

#define LAS __attribute__((address_space(3)))
typedef unsigned short bf16_t;
typedef short bf16x8 __attribute__((ext_vector_type(8)));
typedef short s16x4 __attribute__((ext_vector_type(4)));
typedef short v4i16_t __attribute__((ext_vector_type(4)));
typedef float f32x2 __attribute__((ext_vector_type(2)));
typedef float f32x4 __attribute__((ext_vector_type(4)));
typedef float f32x16 __attribute__((ext_vector_type(16)));
typedef unsigned u32x2 __attribute__((ext_vector_type(2)));
typedef unsigned u32x4 __attribute__((ext_vector_type(4)));
typedef __bf16 bf16x2_t __attribute__((ext_vector_type(2)));

constexpr int SEQ = 2048, DM = 1024, NQKV = 3072, FF = 3584, NH = 32, DH = 32;
constexpr int KTOT = FF + DM;
constexpr size_t WS_FLAG = 0;
constexpr size_t WS_G = 131072;
constexpr size_t WS_XC = 262144;
constexpr size_t WS_Q = 1u << 20;
constexpr size_t WS_K = WS_Q + (4u << 20);
constexpr size_t WS_V = WS_K + (4u << 20);
constexpr size_t WS_A = WS_V + (4u << 20);
constexpr size_t WS_P = WS_A + (20u << 20);

#define WAIT_V(n) asm volatile("s_waitcnt vmcnt(" #n ")" ::: "memory")
#define WAIT_L(n) asm volatile("s_waitcnt lgkmcnt(" #n ")" ::: "memory")
#define BAR() asm volatile("s_barrier" ::: "memory")
#define SCHED() __builtin_amdgcn_sched_barrier(0)
#define PIN1(x) asm volatile("" : "+v"(x))
#define RLX_AGENT __ATOMIC_RELAXED, __HIP_MEMORY_SCOPE_AGENT
#define FINE 0
#define LSTAMP(i) do {} while (0)
#define STAMP(kern, i) do {} while (0)
#define FSTAMP(cond, i) do {} while (0)

__device__ __forceinline__ unsigned cvtpk(float lo, float hi) { f32x2 v = {lo, hi}; bf16x2_t b = __builtin_convertvector(v, bf16x2_t); return __builtin_bit_cast(unsigned, b); }
__device__ __forceinline__ float bf2f(unsigned short b) { return __uint_as_float(((unsigned)b) << 16); }
__device__ __forceinline__ s16x4 trread(LAS const char* p) { return __builtin_bit_cast(s16x4, __builtin_amdgcn_ds_read_tr16_b64_v4i16((LAS v4i16_t*)p)); }
__device__ __forceinline__ bf16x8 cat8(s16x4 lo, s16x4 hi) { return (bf16x8){lo[0], lo[1], lo[2], lo[3], hi[0], hi[1], hi[2], hi[3]}; }
__device__ __forceinline__ void glds16(const char* sbase, unsigned voff, unsigned ldsaddr) {
    asm volatile("s_mov_b32 m0, %2\n\ts_nop 0\n\tglobal_load_lds_dwordx4 %0, %1" :: "v"(voff), "s"(sbase), "s"(ldsaddr) : "memory"); }
__device__ __forceinline__ void glds16_sc1(const char* sbase, unsigned voff, unsigned ldsaddr) {
    asm volatile("s_mov_b32 m0, %2\n\ts_nop 0\n\tglobal_load_lds_dwordx4 %0, %1 sc1" :: "v"(voff), "s"(sbase), "s"(ldsaddr) : "memory"); }
#define LDSP(a) ((LAS const char*)(uintptr_t)(unsigned)(a))
#define CODE_LABEL(name) asm volatile(name ":")
#define CODE_RANGE(base, len, tag, lbeg, lend) do { unsigned long long _pc; int _d; \
    asm volatile("s_getpc_b64 %0\n" tag ":\n\ts_mov_b32 %1, " lbeg "-" tag "\n\ts_mov_b32 %2, " lend "-" lbeg : "=s"(_pc), "=s"(_d), "=s"(len)); \
    base = (const char*)(_pc + (long long)_d); } while (0)
__device__ __forceinline__ float gelu_tanh(float x) {
    const float c0 = -2.0f * 0.7978845608028654f * 1.4426950408889634f, c1 = c0 * 0.044715f;
    const float z = x * __builtin_fmaf(x * x, c1, c0);
    const float e = __builtin_amdgcn_exp2f(z);
    return x * __builtin_amdgcn_rcpf(1.0f + e);
}

__device__ __forceinline__ f32x2 geglu2(f32x2 g, f32x2 u) {
    const float c0 = -2.0f * 0.7978845608028654f * 1.4426950408889634f, c1 = c0 * 0.044715f;
    const f32x2 t = __builtin_elementwise_fma(g * g, (f32x2){c1, c1}, (f32x2){c0, c0});
    const f32x2 z = g * t;
    f32x2 e; e.x = __builtin_amdgcn_exp2f(z.x); e.y = __builtin_amdgcn_exp2f(z.y);
    const f32x2 d = e + (f32x2){1.0f, 1.0f};
    f32x2 r; r.x = __builtin_amdgcn_rcpf(d.x); r.y = __builtin_amdgcn_rcpf(d.y);
    return (g * u) * r;
}

constexpr int K1_GW = 2 * 32768 + 2 * 40960;
constexpr int K1_LDS = K1_GW + 2048;
#define K1_ST_AUX 0
constexpr float QSCALE = 0.17677669529663687f * 1.4426950408889634f;

__global__ __launch_bounds__(512, 2) void k1_gemm(const bf16_t* __restrict__ x, const bf16_t* __restrict__ wqkv, const bf16_t* __restrict__ wg,
                                                  const bf16_t* __restrict__ wu, const bf16_t* __restrict__ gate_w, unsigned char* __restrict__ ws) {
    extern __shared__ __attribute__((aligned(16))) char smem[];
    const unsigned lds = (unsigned)(uintptr_t)(LAS char*)smem;
    const int tid = threadIdx.x, wid = __builtin_amdgcn_readfirstlane(tid >> 6), lane = tid & 63;
    const int wr = wid >> 2, wc = wid & 3, fr = lane & 15, fq = lane >> 4;
    const int bid = blockIdx.x, xcd = bid & 7, idx = bid >> 3, ctl = idx & 3, rb = idx >> 2;
    const bool type1 = ctl < 3;
    const int t1 = xcd * 3 + ctl, t2 = xcd;
    const int gam0 = type1 ? (ctl < 2 ? 2 * xcd + ctl : 16 + 2 * xcd) : 17 + 2 * xcd, gam1 = 32 + 2 * xcd, gam2 = 33 + 2 * xcd;
    const char* gp0; const char* gp1; const char* gp2; const char* gp3; const char* gp4;
    if (type1) {
        gp0 = (const char*)(wg + 128 * t1); gp1 = (const char*)(wg + 128 * t1 + 64); gp2 = (const char*)(wu + 128 * t1); gp3 = (const char*)(wu + 128 * t1 + 64);
        gp4 = (const char*)(wqkv + 64 * gam0);
    } else {
        gp0 = (const char*)(wg + 3072 + 64 * t2); gp1 = (const char*)(wu + 3072 + 64 * t2);
        gp2 = (const char*)(wqkv + 64 * gam0); gp3 = (const char*)(wqkv + 64 * gam1); gp4 = (const char*)(wqkv + 64 * gam2);
    }
    const unsigned ld23 = type1 ? FF * 2 : NQKV * 2;
    const int lr = lane >> 3, ls = lane & 7;
    const unsigned voffA = (unsigned)((8 * wc + lr) * (DM * 2) + ((ls ^ ((4 * (wc & 1) + (lane >> 4)) & 7)) * 16));
    const int sB = 3 * ((lane >> 4) & 1) + 4 * (wc & 1);
    const unsigned voffG = (unsigned)((8 * wc + lr) * (FF * 2) + ((ls ^ sB) * 16));
    const unsigned voffQ = (unsigned)((8 * wc + lr) * (NQKV * 2) + ((ls ^ sB) * 16));
    const unsigned voff23 = type1 ? voffG : voffQ;
    const char* xa = (const char*)x + (size_t)rb * 256 * DM * 2;
#define K1_DMA_A(h, tt, bb) do { const char* _s = xa + (size_t)(h) * 128 * DM * 2 + (size_t)(tt) * 128; \
        _Pragma("unroll") for (int _u = 0; _u < 4; ++_u) glds16(_s + (size_t)_u * 32 * DM * 2, voffA, lds + (bb) * 32768 + ((h) * 128 + _u * 32 + wc * 8) * 128); } while (0)
#define K1_DMA_B(hb, tt, bb) do { const size_t _k = (size_t)(tt) * 64 + (hb) * 32; const unsigned _d = lds + 65536 + (bb) * 40960 + ((hb) * 32 + wc * 8) * 128; \
        glds16(gp0 + _k * (FF * 2), voffG, _d); glds16(gp1 + _k * (FF * 2), voffG, _d + 8192); \
        glds16(gp2 + _k * ld23, voff23, _d + 16384); glds16(gp3 + _k * ld23, voff23, _d + 24576); \
        glds16(gp4 + _k * (NQKV * 2), voffQ, _d + 32768); } while (0)

    STAMP(0, 0);
    const unsigned gscr = lds + 65536 + 40960 + (32 + wc * 8) * 128;
    if (wr == 1) {
        const char* xr = (const char*)x + (size_t)(rb * 256 + (xcd * 4 + ctl) * 8 + wc * 2) * (DM * 2);
#pragma unroll
        for (int i = 0; i < 4; ++i) glds16(xr + i * 1024, (unsigned)(lane * 16), gscr + i * 8192);
    }
    if (wid >= 6) glds16((const char*)gate_w + (wid - 6) * 1024, (unsigned)(lane * 16), lds + K1_GW + (wid - 6) * 1024);
    if (wr == 0) { K1_DMA_B(0, 0, 0); K1_DMA_A(1, 0, 0); K1_DMA_B(0, 1, 1); }
    else         { K1_DMA_A(0, 0, 0); K1_DMA_B(1, 0, 0); K1_DMA_A(0, 1, 1); }

    const unsigned aoff0 = lds + (wr * 128 + fr) * 128 + ((fq ^ (fr >> 1)) * 16);
    const unsigned aoff1 = lds + (wr * 128 + fr) * 128 + (((fq ^ (fr >> 1)) ^ 4) * 16);
    const int q4 = (lane >> 2) & 3, p4 = lane & 3;
    const int sR = 3 * ((q4 >> 1) & 1) + 4 * (fq & 1);
    const int krow = (8 * fq + q4) * 128;
    int gA, cA, gB, cB, gS;
    if (type1) { gA = wc >> 1; cA = 4 * (wc & 1); gB = 2 + (wc >> 1); cB = cA; gS = 4; }
    else if (wc < 2) { gA = 0; cA = 4 * wc; gB = 1; cB = cA; gS = 2; }
    else { gA = wc + 1; cA = 0; gB = wc + 1; cB = 4; gS = 2; }
    const unsigned boffA0 = lds + 65536 + gA * 8192 + krow + (((cA + p4) ^ sR) * 16) + (p4 & 1) * 8;
    const unsigned boffA1 = lds + 65536 + gA * 8192 + krow + (((cA + p4) ^ sR) * 16) + ((p4 & 1) ^ 1) * 8;
    const unsigned boffB0 = lds + 65536 + gB * 8192 + krow + (((cB + p4) ^ sR) * 16) + (p4 & 1) * 8;
    const unsigned boffB1 = lds + 65536 + gB * 8192 + krow + (((cB + p4) ^ sR) * 16) + ((p4 & 1) ^ 1) * 8;
    const unsigned boffS = lds + 65536 + gS * 8192 + krow + (((2 * wc + (p4 >> 1)) ^ sR) * 16) + (p4 & 1) * 8;

    f32x4 acc[8][5];
#pragma unroll
    for (int m = 0; m < 8; ++m)
#pragma unroll
        for (int n = 0; n < 5; ++n) acc[m][n] = (f32x4){0.f, 0.f, 0.f, 0.f};
    bf16x8 af[8], bfg[5];

    WAIT_V(9); BAR();
    STAMP(0, 1);
    if (wr == 1) {
        const u32x4 w0 = *(LAS const u32x4*)(LDSP(lds + K1_GW) + lane * 16), w1 = *(LAS const u32x4*)(LDSP(lds + K1_GW) + 1024 + lane * 16);
#pragma unroll
        for (int r = 0; r < 2; ++r) {
            const u32x4 x0 = *(LAS const u32x4*)(LDSP(gscr) + (2 * r) * 8192 + lane * 16), x1 = *(LAS const u32x4*)(LDSP(gscr) + (2 * r + 1) * 8192 + lane * 16);
            float sacc = 0.f;
#pragma unroll
            for (int i = 0; i < 4; ++i) {
                sacc = __builtin_fmaf(__uint_as_float(x0[i] << 16), __uint_as_float(w0[i] << 16), sacc);
                sacc = __builtin_fmaf(__uint_as_float(x0[i] & 0xffff0000u), __uint_as_float(w0[i] & 0xffff0000u), sacc);
                sacc = __builtin_fmaf(__uint_as_float(x1[i] << 16), __uint_as_float(w1[i] << 16), sacc);
                sacc = __builtin_fmaf(__uint_as_float(x1[i] & 0xffff0000u), __uint_as_float(w1[i] & 0xffff0000u), sacc);
            }
#pragma unroll
            for (int o = 32; o >= 1; o >>= 1) sacc += __shfl_xor(sacc, o);
            if (lane == 0) ((float*)(ws + WS_G))[rb * 256 + (xcd * 4 + ctl) * 8 + wc * 2 + r] = 1.0f / (1.0f + __expf(-sacc));
        }
        WAIT_L(0);
        BAR();
    }

#define K1_SEG(b, ks, t) do { \
        { constexpr int _bo = (b) * 40960 + (ks) * 4096; \
          bfg[0] = cat8(trread(LDSP(boffA0) + _bo), trread(LDSP(boffA0) + _bo + 512)); bfg[1] = cat8(trread(LDSP(boffA1) + _bo), trread(LDSP(boffA1) + _bo + 512)); \
          bfg[2] = cat8(trread(LDSP(boffB0) + _bo), trread(LDSP(boffB0) + _bo + 512)); bfg[3] = cat8(trread(LDSP(boffB1) + _bo), trread(LDSP(boffB1) + _bo + 512)); \
          bfg[4] = cat8(trread(LDSP(boffS) + _bo), trread(LDSP(boffS) + _bo + 512)); \
          LAS const char* _ap = LDSP((ks) ? aoff1 : aoff0) + (b) * 32768; \
          _Pragma("unroll") for (int _m = 0; _m < 8; ++_m) af[_m] = *(LAS const bf16x8*)(_ap + _m * 2048); } \
        if ((ks) == 0) { const int _tt = ((t) + 1 < 16) ? (t) + 1 : 15; if (wr == 0) K1_DMA_A(1, _tt, (b) ^ 1); else K1_DMA_B(1, _tt, (b) ^ 1); } \
        else { const int _tt = ((t) + 2 < 16) ? (t) + 2 : 15; if (wr == 0) K1_DMA_B(0, _tt, (b)); else K1_DMA_A(0, _tt, (b)); } \
        WAIT_V(9); WAIT_L(0); SCHED(); BAR(); SCHED(); \
        __builtin_amdgcn_s_setprio(1); \
        _Pragma("unroll") for (int _m = 0; _m < 8; ++_m) _Pragma("unroll") for (int _n = 0; _n < 5; ++_n) \
            acc[_m][_n] = __builtin_amdgcn_mfma_f32_16x16x32_bf16(bfg[_n], af[_m], acc[_m][_n], 0, 0, 0); \
        __builtin_amdgcn_s_setprio(0); \
        SCHED(); if (!((t) == 15 && (ks) == 1 && wr == 1)) BAR(); SCHED(); } while (0)

    for (int t = 0; t < 16; t += 2) {
        K1_SEG(0, 0, t); K1_SEG(0, 1, t); K1_SEG(1, 0, t + 1); K1_SEG(1, 1, t + 1);
    }
    STAMP(0, 2);

    const bool roleP = type1 || wc < 2;
    int lane_e = lane; asm volatile("" : "+v"(lane_e));
    {
    const int fr = lane_e & 15, fq = lane_e >> 4;
    const int row0 = rb * 256 + wr * 128 + fr;
    const bool odd = fq & 1;
    const __amdgpu_buffer_rsrc_t rs = __builtin_amdgcn_make_buffer_rsrc((void*)ws, (short)0, (int)(64u << 20), 0x00020000);
#define K1_QKV_ADDR(col0, dl, voff, step, sc) \
        const int _part = (col0) >> 10, _head = ((col0) & 1023) >> 5, _d = ((col0) & 31) + (dl); \
        const float sc = _part == 0 ? QSCALE : 1.0f; \
        unsigned voff; unsigned step; \
        if (_part == 1) { voff = (unsigned)(WS_K + ((size_t)_head * 65536 + (row0 >> 6) * 2048 + (row0 & 63) * 8) * 2) + (unsigned)(((_d >> 3) * 512 + (_d & 7)) * 2); step = 256u; } \
        else { voff = (unsigned)((_part == 0 ? WS_Q : WS_V) + ((size_t)_head * SEQ) * 64) + (unsigned)(row0 * 64 + _d * 2); step = 1024u; }
#define K1_ROWOFF(part1, m) ((part1) ? (unsigned)(((m) >> 2) * 4096 + ((m) & 3) * 256) : (unsigned)((m) * 1024))
    {
        const int col0 = 64 * gam0 + 16 * wc;
        K1_QKV_ADDR(col0, 4 * fq, voff, step, sc);
        const bool isK = step == 256u;
#pragma unroll
        for (int m = 0; m < 8; ++m) {
            const f32x4 v = acc[m][4] * sc;
            u32x2 w; w.x = cvtpk(v[0], v[1]); w.y = cvtpk(v[2], v[3]);
            __builtin_amdgcn_raw_buffer_store_b64(w, rs, voff + (isK ? K1_ROWOFF(true, m) : K1_ROWOFF(false, m)), 0, K1_ST_AUX);
        }
    }
    if (roleP) {
        const int hcol = (type1 ? 128 * t1 : 3072 + 64 * t2) + 32 * wc + 8 * fq;
        const unsigned voff = (unsigned)WS_A + (unsigned)(row0 * (KTOT * 2) + hcol * 2);
#pragma unroll
        for (int m = 0; m < 8; ++m) {
            const f32x2 h00 = geglu2((f32x2){acc[m][0][0], acc[m][0][1]}, (f32x2){acc[m][2][0], acc[m][2][1]}), h01 = geglu2((f32x2){acc[m][0][2], acc[m][0][3]}, (f32x2){acc[m][2][2], acc[m][2][3]});
            const f32x2 h10 = geglu2((f32x2){acc[m][1][0], acc[m][1][1]}, (f32x2){acc[m][3][0], acc[m][3][1]}), h11 = geglu2((f32x2){acc[m][1][2], acc[m][1][3]}, (f32x2){acc[m][3][2], acc[m][3][3]});
            const unsigned a0 = cvtpk(h00.x, h00.y), a1 = cvtpk(h01.x, h01.y), b0 = cvtpk(h10.x, h10.y), b1 = cvtpk(h11.x, h11.y);
            u32x4 w; w.x = odd ? b0 : a0; w.y = odd ? b1 : a1; w.z = odd ? a0 : b0; w.w = odd ? a1 : b1;
            __builtin_amdgcn_raw_buffer_store_b128(w, rs, voff + (unsigned)(m * 16 * KTOT * 2), 0, K1_ST_AUX);
        }
    } else {
#pragma unroll
        for (int pr = 0; pr < 2; ++pr) {
            const int col0 = 64 * (wc == 2 ? gam1 : gam2) + 32 * pr;
            K1_QKV_ADDR(col0, 8 * fq, voff, step, sc);
            const bool isK = step == 256u;
#pragma unroll
            for (int m = 0; m < 8; ++m) {
                const f32x4 v0 = acc[m][2 * pr] * sc, v1 = acc[m][2 * pr + 1] * sc;
                const unsigned a0 = cvtpk(v0[0], v0[1]), a1 = cvtpk(v0[2], v0[3]), b0 = cvtpk(v1[0], v1[1]), b1 = cvtpk(v1[2], v1[3]);
                u32x4 w; w.x = odd ? b0 : a0; w.y = odd ? b1 : a1; w.z = odd ? a0 : b0; w.w = odd ? a1 : b1;
                __builtin_amdgcn_raw_buffer_store_b128(w, rs, voff + (isK ? K1_ROWOFF(true, m) : K1_ROWOFF(false, m)), 0, K1_ST_AUX);
            }
        }
    }
#undef K1_QKV_ADDR
#undef K1_ROWOFF
    }
    if (tid == 0) ((unsigned*)(ws + WS_FLAG))[bid * 64] = 0u;
    if (tid == 64 && bid < 32) ((unsigned*)(ws + WS_FLAG))[(256 + bid) * 64] = 0u;
    if (tid >= 128 && tid < 136) ((unsigned*)(ws + WS_XC))[(bid * 8 + (tid - 128)) * 32] = 0u;
    STAMP(0, 3);
    WAIT_V(0);
    STAMP(0, 4);
#undef K1_SEG
#undef K1_DMA_A
#undef K1_DMA_B
}

#define OSC1 1
constexpr int K3_STG = 49152;
constexpr int K23_SCR = 3 * K3_STG;
constexpr int K23_LDS = K23_SCR + 1024;
#define K2_RING(j) (K3_STG + (((j) + 1) % 3) * 32768)
__device__ __forceinline__ int crow(int r, int hi) { return (r & 3) + 8 * (r >> 2) + 4 * hi; }
#define MFMA32(a, b, c) __builtin_amdgcn_mfma_f32_32x32x16_bf16(a, b, c, 0, 0, 0)
#define MFMA16(a, b, c) __builtin_amdgcn_mfma_f32_16x16x32_bf16(a, b, c, 0, 0, 0)
#define MX3(a, b, c) __builtin_fmaxf(__builtin_fmaxf((a), (b)), (c))
#define PIN(x) asm volatile("" : "+v"(x))
#define EX2(v) __builtin_amdgcn_exp2f(v)
__device__ __forceinline__ void glds4_sc1(const char* sbase, unsigned voff, unsigned ldsaddr) {
    asm volatile("s_mov_b32 m0, %2\n\ts_nop 0\n\tglobal_load_lds_dword %0, %1 sc1" :: "v"(voff), "s"(sbase), "s"(ldsaddr) : "memory"); }
__device__ __forceinline__ void glds4(const char* sbase, unsigned voff, unsigned ldsaddr) {
    asm volatile("s_mov_b32 m0, %2\n\ts_nop 0\n\tglobal_load_lds_dword %0, %1" :: "v"(voff), "s"(sbase), "s"(ldsaddr) : "memory"); }

__global__ __launch_bounds__(768, 3) void k23(const bf16_t* __restrict__ wo, const bf16_t* __restrict__ wd, unsigned char* __restrict__ ws, bf16_t* __restrict__ out) {
    extern __shared__ __attribute__((aligned(16))) char smem[];
    const unsigned lds = (unsigned)(uintptr_t)(LAS char*)smem;
    const int tid = threadIdx.x, wid = __builtin_amdgcn_readfirstlane(tid >> 6), lane = tid & 63;
    const int bid = blockIdx.x, xcd = bid & 7, idx = bid >> 3;
    unsigned* flags = (unsigned*)(ws + WS_FLAG);
    const int kq = xcd >> 1, mb = (xcd & 1) * 4 + (idx >> 3), nb = idx & 7;
    const int head = xcd * 4 + (idx & 3), qb = idx >> 2;
    LAS unsigned* fl = (LAS unsigned*)(uintptr_t)(lds + K23_SCR);
    CODE_LABEL("gm_k23_beg");

    if (wid >= 8) {
        const int L = wid - 8;
        const int lr = lane >> 3, ls = lane & 7;
        const char* Kg = (const char*)(ws + WS_K) + (size_t)head * 131072;
        const char* Vg = (const char*)(ws + WS_V) + (size_t)head * 131072;
        const unsigned voff = (unsigned)(L * 4096 + lane * 16);
#define K2_DMA(j) do { const char* _k = Kg + (size_t)(j) * 16384; const char* _v = Vg + (size_t)(j) * 16384; const unsigned _d = lds + K2_RING(j) + L * 4096; \
        _Pragma("unroll") for (int _i = 0; _i < 4; ++_i) glds16(_k + _i * 1024, voff, _d + _i * 1024); \
        _Pragma("unroll") for (int _i = 0; _i < 4; ++_i) glds16(_v + _i * 1024, voff, _d + 16384 + _i * 1024); } while (0)
        const unsigned voffA0 = (unsigned)(lr * (KTOT * 2) + ((ls ^ ((lane >> 4) & 7)) * 16)), voffA1 = (unsigned)(lr * (KTOT * 2) + ((ls ^ ((4 + (lane >> 4)) & 7)) * 16));
        const unsigned voffB0 = (unsigned)(lr * (DM * 2) + ((ls ^ (3 * ((lane >> 4) & 1))) * 16)), voffB1 = (unsigned)(lr * (DM * 2) + ((ls ^ (3 * ((lane >> 4) & 1) + 4)) * 16));
        const char* Ag = (const char*)(ws + WS_A) + (size_t)(mb * 256 + L * 32) * KTOT * 2;
#define K3_DMA_A(tt, h) do { const int _t = (tt) < 18 ? (tt) : 17; const unsigned _d = lds + ((tt) % 3) * K3_STG + (h) * 16384 + L * 4096; \
        const char* _s = Ag + (_t < 14 ? (14 * kq + _t) * 128 : FF * 2 + (4 * kq + _t - 14) * 128) + (size_t)((h) * 128) * KTOT * 2; \
        if (!OSC1 || _t < 14) { glds16(_s, voffA0, _d); glds16(_s + (size_t)8 * KTOT * 2, voffA1, _d + 1024); glds16(_s + (size_t)16 * KTOT * 2, voffA0, _d + 2048); glds16(_s + (size_t)24 * KTOT * 2, voffA1, _d + 3072); } \
        else { glds16_sc1(_s, voffA0, _d); glds16_sc1(_s + (size_t)8 * KTOT * 2, voffA1, _d + 1024); glds16_sc1(_s + (size_t)16 * KTOT * 2, voffA0, _d + 2048); glds16_sc1(_s + (size_t)24 * KTOT * 2, voffA1, _d + 3072); } } while (0)
#define K3_DMA_B(tt, g) do { const int _t = (tt) < 18 ? (tt) : 17; const unsigned _d = lds + ((tt) % 3) * K3_STG + 32768 + (g) * 8192 + L * 2048; \
        const char* _w = (_t < 14 ? (const char*)wd + (size_t)(14 * kq + _t) * 64 * DM * 2 : (const char*)wo + (size_t)(4 * kq + _t - 14) * 64 * DM * 2) + nb * 256 + (g) * 128 + (size_t)(L * 16) * DM * 2; \
        glds16(_w, voffB0, _d); glds16(_w + (size_t)8 * DM * 2, voffB1, _d + 1024); } while (0)
        K2_DMA(0); K2_DMA(1);
        {
            const char* cb; int cl;
            CODE_RANGE(cb, cl, "gm_k23_ref", "gm_k23_beg", "gm_k23_end");
            const int off = ((L & 1) * 64 + lane) * 128;
            if (L >= 2) CODE_RANGE(cb, cl, "gm_k23_ref2", "gm_k23_ref2", "gm_k23_end2");
            if (cl > 0) glds4(cb, (unsigned)(off < cl - 4 ? off : 0), lds + K23_SCR + 256);
        }
        WAIT_V(0); BAR();
#pragma unroll
        for (int j = 0; j < 8; ++j) {
            if (j == 1 || j == 2) { WAIT_V(0); BAR(); } else if (j >= 3 && j <= 5) { WAIT_V(1); BAR(); } else if (j == 6) { WAIT_V(13); BAR(); } else if (j == 7) { BAR(); }
            if (j + 2 < 8) K2_DMA(j + 2);
            if (j >= 2 && j <= 5) {
                const int ins = (j - 2) * 4 + L;
                if (ins < 9) {
                    const int row0 = (idx * 9 + ins) * 4;
                    const char* base = row0 < 896 ? (const char*)wd + (size_t)(896 * kq + row0) * 2048 : (const char*)wo + (size_t)(256 * kq + row0 - 896) * 2048;
                    glds4(base, (unsigned)(lane * 128), lds + K23_SCR + 256);
                } else {
                    const int e = ins < 14 ? ins - 9 : ins - 11;
                    const int x = e * 32 + idx;
                    const char* base = (const char*)(ws + WS_A) + (size_t)((xcd & 1) * 1024 + (x & 15) * 64) * (KTOT * 2) + (14 * kq + (x >> 4)) * 128;
                    glds4(base, (unsigned)(lane * (KTOT * 2)), lds + K23_SCR + 256);
                }
            }
            if (j == 5) { K3_DMA_A(0, 0); K3_DMA_B(0, 0); K3_DMA_B(0, 1); K3_DMA_A(0, 1); }
            if (j == 6) { K3_DMA_A(1, 0); K3_DMA_A(1, 1); }
            if (j == 7) { K3_DMA_B(1, 0); K3_DMA_B(1, 1); }
        }
        WAIT_V(0); BAR();
        const unsigned redo = __builtin_amdgcn_readfirstlane(__any(fl[lane & 7] != 0u) ? 1 : 0);
        if (redo) {
            K2_DMA(0); K2_DMA(1);
            for (int j = 0; j < 8; ++j) {
                if (j < 7) WAIT_V(8); else WAIT_V(0);
                BAR();
                if (j + 2 < 8) K2_DMA(j + 2);
            }
            BAR();
            K3_DMA_A(1, 0); K3_DMA_B(1, 0); K3_DMA_B(1, 1); K3_DMA_A(1, 1);
            WAIT_V(0); BAR();
        }
#define K3_DMA_T(tt) do { K3_DMA_A(tt, 0); K3_DMA_B(tt, 0); K3_DMA_B(tt, 1); K3_DMA_A(tt, 1); } while (0)
        K3_DMA_T(2);
        int ost = 0;
        for (int t = 0; t < 18; ++t) {
            if (L == 0 && t >= 4 && t <= 11) {
                if (t == 8 || t == 10 || t == 11) {
                    WAIT_V(12);
                    if (ost == 0) {
                        if ((unsigned)__builtin_amdgcn_readfirstlane(fl[128]) >= 8u) ost = 2;
                        else if (t == 11) {
                            unsigned spins = 0;
                            while (__builtin_amdgcn_readfirstlane(__hip_atomic_load(flags + (256 + mb * 4 + kq) * 64, RLX_AGENT)) < 8u) { __builtin_amdgcn_s_sleep(1); if (++spins > (1u << 22)) break; }
                            ost = 2;
                        }
                        if (OSC1 && ost == 2) ost = 1;
                        if (ost == 2 && t == 11) { __builtin_amdgcn_fence(__ATOMIC_ACQUIRE, "agent"); WAIT_V(0); ost = 1; }
                    } else if (t == 11 && ost == 3) WAIT_V(0);
                } else WAIT_V(13);
            } else if (t < 16) WAIT_V(12);
            else WAIT_V(0);
            BAR();
            if (t + 3 < 18) K3_DMA_T(t + 3);
            if (L == 0) {
                if (t == 3) { if (lane == 0) __hip_atomic_fetch_add(flags + (256 + qb * 4 + (xcd >> 1)) * 64, 1u, RLX_AGENT); }
                else if (t == 5 || (t == 8 && ost == 0)) glds4_sc1((const char*)(flags + (256 + mb * 4 + kq) * 64), 0u, lds + K23_SCR + 512);
                else if (ost == 2) { asm volatile("buffer_inv sc1" ::: "memory"); ost = (t == 10) ? 3 : 1; }
            }
        }
#undef K3_DMA_T
        CODE_LABEL("gm_k23_end2");
        return;
#undef K2_DMA
#undef K3_DMA_A
#undef K3_DMA_B
    }

    STAMP(1, 0);
    float omg[4];
    { const float* gv = (const float*)(ws + WS_G) + mb * 256 + (wid >> 2) * 128 + ((wid & 3) >> 1) * 64 + (lane & 15);
#pragma unroll
      for (int m = 0; m < 4; ++m) omg[m] = 1.0f - gv[16 * m]; }

    {
        const int r32 = lane & 31, hi = lane >> 5;
        const bf16_t* Qg = (const bf16_t*)(ws + WS_Q) + ((size_t)head * SEQ + qb * 256 + wid * 32) * 32;
        bf16x8 qf0 = *(const bf16x8*)(Qg + r32 * 32 + hi * 8);
        bf16x8 qf1 = *(const bf16x8*)(Qg + r32 * 32 + 16 + hi * 8);
        float gval = ((const float*)(ws + WS_G))[qb * 256 + wid * 32 + r32];
        bf16x8 ones;
        { const short o1 = (((lane >> 4) & 1) == (((lane & 15) >> 2) & 1)) ? (short)0x3F80 : (short)0;
#pragma unroll
          for (int j = 0; j < 8; ++j) ones[j] = o1; }
        const unsigned koff = lds + hi * 1024 + r32 * 16;
        const unsigned voffr = lds + 16384 + ((lane >> 4) & 1) * 32 + (lane & 3) * 8 + (4 * hi + ((lane & 15) >> 2)) * 64;
        const unsigned koff0 = koff + K2_RING(0);
        f32x16 o = {}; f32x4 lsum = {0.f, 0.f, 0.f, 0.f};
        asm volatile("" : "+v"(omg[0]), "+v"(omg[1]), "+v"(omg[2]), "+v"(omg[3]), "+v"(gval), "+v"(qf0), "+v"(qf1));
        BAR();
        STAMP(1, 1);
        {
            f32x16 SA, SB, negm;
            u32x4 WA0, WA1, WB0 = {0u, 0u, 0u, 0u}, WB1 = {0u, 0u, 0u, 0u};
            bf16x8 kf0, kf1, vf0 = {}, vf1 = {};
            kf0 = *(LAS const bf16x8*)(LDSP(koff0)); kf1 = *(LAS const bf16x8*)(LDSP(koff0) + 2048);
            { f32x16 z = {}; SA = MFMA32(kf0, qf0, z); SA = MFMA32(kf1, qf1, SA); }
            kf0 = *(LAS const bf16x8*)(LDSP(koff0) + 512); kf1 = *(LAS const bf16x8*)(LDSP(koff0) + 2048 + 512);
            {
                float a = MX3(SA[0], SA[1], SA[2]), b = MX3(SA[3], SA[4], SA[5]);
                a = MX3(a, SA[6], SA[7]); b = MX3(b, SA[8], SA[9]); a = MX3(a, SA[10], SA[11]); b = MX3(b, SA[12], SA[13]); a = MX3(a, SA[14], SA[15]);
                float rm = __builtin_fmaxf(a, b);
                auto rr = __builtin_amdgcn_permlane32_swap(__float_as_uint(rm), __float_as_uint(rm), false, false); rm = __builtin_fmaxf(__uint_as_float(rr[0]), __uint_as_float(rr[1]));
#pragma unroll
                for (int r = 0; r < 16; ++r) { SA[r] -= rm; negm[r] = -rm; }
                PIN(negm);
            }
#define K2_STEP(C, N, WP0, WP1, WC0, WC1, KADDR, VADDR) do { \
            N = MFMA32(kf0, qf0, negm); \
            C[0] = EX2(C[0]); C[1] = EX2(C[1]); C[2] = EX2(C[2]); C[3] = EX2(C[3]); PIN(C); SCHED(); \
            kf0 = *(LAS const bf16x8*)(KADDR); SCHED(); \
            o = MFMA32(vf0, __builtin_bit_cast(bf16x8, WP0), o); \
            C[4] = EX2(C[4]); C[5] = EX2(C[5]); C[6] = EX2(C[6]); C[7] = EX2(C[7]); PIN(C); SCHED(); \
            vf0 = cat8(trread(VADDR), trread((VADDR) + 512)); SCHED(); \
            lsum = MFMA16(ones, __builtin_bit_cast(bf16x8, WP0), lsum); \
            WC0[0] = cvtpk(C[0], C[1]); WC0[1] = cvtpk(C[2], C[3]); WC0[2] = cvtpk(C[4], C[5]); WC0[3] = cvtpk(C[6], C[7]); PIN(WC0); SCHED(); \
            N = MFMA32(kf1, qf1, N); \
            C[8] = EX2(C[8]); C[9] = EX2(C[9]); C[10] = EX2(C[10]); C[11] = EX2(C[11]); PIN(C); SCHED(); \
            kf1 = *(LAS const bf16x8*)((KADDR) + 2048); SCHED(); \
            o = MFMA32(vf1, __builtin_bit_cast(bf16x8, WP1), o); \
            C[12] = EX2(C[12]); C[13] = EX2(C[13]); C[14] = EX2(C[14]); C[15] = EX2(C[15]); PIN(C); SCHED(); \
            vf1 = cat8(trread((VADDR) + 1024), trread((VADDR) + 1536)); SCHED(); \
            lsum = MFMA16(ones, __builtin_bit_cast(bf16x8, WP1), lsum); \
            WC1[0] = cvtpk(C[8], C[9]); WC1[1] = cvtpk(C[10], C[11]); WC1[2] = cvtpk(C[12], C[13]); WC1[3] = cvtpk(C[14], C[15]); PIN(WC1); SCHED(); \
            } while (0)
            for (int j = 0; j < 8; ++j) {
                if (j > 0) { WAIT_L(0); BAR(); }
                LAS const char* kc = LDSP(koff + K2_RING(j));
                LAS const char* kn = LDSP(koff + K2_RING(j + 1));
                LAS const char* vc = LDSP(voffr + K2_RING(j));
#define K2_PRIO(s_) do {} while (0)
                K2_PRIO(0); K2_STEP(SA, SB, WB0, WB1, WA0, WA1, kc + 1 * 4096, vc + 0 * 2048);
                K2_PRIO(1); K2_STEP(SB, SA, WA0, WA1, WB0, WB1, kc + 1 * 4096 + 512, vc + 1 * 2048);
                K2_PRIO(2); K2_STEP(SA, SB, WB0, WB1, WA0, WA1, kc + 2 * 4096, vc + 2 * 2048);
                K2_PRIO(3); K2_STEP(SB, SA, WA0, WA1, WB0, WB1, kc + 2 * 4096 + 512, vc + 3 * 2048);
                K2_PRIO(4); K2_STEP(SA, SB, WB0, WB1, WA0, WA1, kc + 3 * 4096, vc + 4 * 2048);
                K2_PRIO(5); K2_STEP(SB, SA, WA0, WA1, WB0, WB1, kc + 3 * 4096 + 512, vc + 5 * 2048);
                K2_PRIO(6); K2_STEP(SA, SB, WB0, WB1, WA0, WA1, kn, vc + 6 * 2048);
                K2_PRIO(7); K2_STEP(SB, SA, WA0, WA1, WB0, WB1, kn + 512, vc + 7 * 2048);
            }
            o = MFMA32(vf0, __builtin_bit_cast(bf16x8, WB0), o); lsum = MFMA16(ones, __builtin_bit_cast(bf16x8, WB0), lsum);
            o = MFMA32(vf1, __builtin_bit_cast(bf16x8, WB1), o); lsum = MFMA16(ones, __builtin_bit_cast(bf16x8, WB1), lsum);
#undef K2_STEP
        }
        STAMP(1, 2);
        const __amdgpu_buffer_rsrc_t rso = __builtin_amdgcn_make_buffer_rsrc((void*)(ws + WS_A), (short)0, (int)(SEQ * KTOT * 2), 0x00020000);
        const unsigned ob = (unsigned)(((qb * 256 + wid * 32 + r32) * KTOT + FF + head * 32 + 8 * hi) * 2);
#define K2_STORE_O() do { \
            const float sc = gval * __builtin_amdgcn_rcpf(lsum[0]); \
            unsigned w[8]; \
            _Pragma("unroll") for (int g = 0; g < 4; ++g) { w[2 * g] = cvtpk(o[4 * g] * sc, o[4 * g + 1] * sc); w[2 * g + 1] = cvtpk(o[4 * g + 2] * sc, o[4 * g + 3] * sc); } \
            _Pragma("unroll") for (int pr = 0; pr < 2; ++pr) { \
                auto r0 = __builtin_amdgcn_permlane32_swap(w[4 * pr], w[4 * pr + 2], false, false); \
                auto r1 = __builtin_amdgcn_permlane32_swap(w[4 * pr + 1], w[4 * pr + 3], false, false); \
                const u32x4 v = {r0[0], r1[0], r0[1], r1[1]}; \
                __builtin_amdgcn_raw_buffer_store_b128(v, rso, ob + pr * 32, 0, 16); } } while (0)
        {
            const bool bad = !(lsum[0] <= 1.0e9f);
            const unsigned badw = __any(bad) ? 1u : 0u;
            if (lane == 0) fl[wid] = badw;
            K2_STORE_O();
            STAMP(1, 3);
            WAIT_L(0); BAR();
            const unsigned redo = __builtin_amdgcn_readfirstlane(__any(fl[lane & 7] != 0u) ? 1 : 0);
            if (redo) {
                o = (f32x16){}; lsum = (f32x4){0.f, 0.f, 0.f, 0.f};
                float mhat = 0.f; f32x16 negm = {};
                bool first = true;
                for (int j = 0; j < 8; ++j) {
                    WAIT_L(0); BAR();
                    LAS const char* kb = LDSP(koff + K2_RING(j));
                    LAS const char* vb = LDSP(voffr + K2_RING(j));
#pragma unroll 1
                    for (int tt = 0; tt < 4; ++tt) {
                        LAS const char* kp = kb + tt * 4096;
                        LAS const char* vp = vb + tt * 4096;
                        const bf16x8 k00 = *(LAS const bf16x8*)(kp), k01 = *(LAS const bf16x8*)(kp + 2048), k10 = *(LAS const bf16x8*)(kp + 512), k11 = *(LAS const bf16x8*)(kp + 2048 + 512);
                        f32x16 p0 = MFMA32(k00, qf0, negm); p0 = MFMA32(k01, qf1, p0);
                        f32x16 p1 = MFMA32(k10, qf0, negm); p1 = MFMA32(k11, qf1, p1);
                        float a = MX3(p0[0], p0[1], p1[0]), b = MX3(p0[2], p0[3], p1[1]); a = MX3(a, p1[2], p1[3]);
#pragma unroll
                        for (int r = 4; r < 16; r += 4) { a = MX3(a, p0[r], p0[r + 1]); b = MX3(b, p0[r + 2], p0[r + 3]); a = MX3(a, p1[r], p1[r + 1]); b = MX3(b, p1[r + 2], p1[r + 3]); }
                        float rm = __builtin_fmaxf(a, b);
                        { auto rr = __builtin_amdgcn_permlane32_swap(__float_as_uint(rm), __float_as_uint(rm), false, false); rm = __builtin_fmaxf(__uint_as_float(rr[0]), __uint_as_float(rr[1])); }
                        {
                            const float dl = first ? rm : __builtin_fmaxf(rm, 0.f);
                            mhat += dl;
#pragma unroll
                            for (int r = 0; r < 16; ++r) { p0[r] -= dl; p1[r] -= dl; negm[r] = -mhat; }
                            if (!first) { const float f = EX2(-dl); o = o * f; lsum = lsum * f; }
                            first = false;
                        }
#pragma unroll
                        for (int r = 0; r < 16; ++r) { p0[r] = EX2(p0[r]); p1[r] = EX2(p1[r]); }
                        const u32x4 w0 = {cvtpk(p0[0], p0[1]), cvtpk(p0[2], p0[3]), cvtpk(p0[4], p0[5]), cvtpk(p0[6], p0[7])};
                        const u32x4 w1 = {cvtpk(p0[8], p0[9]), cvtpk(p0[10], p0[11]), cvtpk(p0[12], p0[13]), cvtpk(p0[14], p0[15])};
                        const u32x4 w2 = {cvtpk(p1[0], p1[1]), cvtpk(p1[2], p1[3]), cvtpk(p1[4], p1[5]), cvtpk(p1[6], p1[7])};
                        const u32x4 w3 = {cvtpk(p1[8], p1[9]), cvtpk(p1[10], p1[11]), cvtpk(p1[12], p1[13]), cvtpk(p1[14], p1[15])};
                        const bf16x8 v0 = cat8(trread(vp), trread(vp + 512)), v1 = cat8(trread(vp + 1024), trread(vp + 1536));
                        const bf16x8 v2 = cat8(trread(vp + 2048), trread(vp + 2560)), v3 = cat8(trread(vp + 3072), trread(vp + 3584));
                        const bf16x8 f0 = __builtin_bit_cast(bf16x8, w0), f1 = __builtin_bit_cast(bf16x8, w1), f2 = __builtin_bit_cast(bf16x8, w2), f3 = __builtin_bit_cast(bf16x8, w3);
                        o = MFMA32(v0, f0, o); lsum = MFMA16(ones, f0, lsum);
                        o = MFMA32(v1, f1, o); lsum = MFMA16(ones, f1, lsum);
                        o = MFMA32(v2, f2, o); lsum = MFMA16(ones, f2, lsum);
                        o = MFMA32(v3, f3, o); lsum = MFMA16(ones, f3, lsum);
                    }
                }
                WAIT_L(0); BAR();
                WAIT_V(0);
                K2_STORE_O();
                BAR();
            }
        }
        STAMP(1, 4);
#undef K2_STORE_O
    }

    {
        const int G = wid >> 2, j4 = wid & 3, wm = j4 >> 1, wn = j4 & 1, fr = lane & 15, fq = lane >> 4;
        const int arow = G * 128 + wm * 64 + fr;
        const unsigned aoff0 = lds + arow * 128 + ((fq ^ (fr >> 1)) * 16);
        const unsigned aoff1 = lds + arow * 128 + (((fq ^ (fr >> 1)) ^ 4) * 16);
        const int q4 = (lane >> 2) & 3, p4 = lane & 3;
        const int sR = 3 * ((q4 >> 1) & 1) + 4 * (fq & 1);
        const int krow = (8 * fq + q4) * 128;
        const unsigned boff00 = lds + 32768 + wn * 8192 + krow + ((p4 ^ sR) * 16) + (p4 & 1) * 8;
        const unsigned boff01 = lds + 32768 + wn * 8192 + krow + ((p4 ^ sR) * 16) + ((p4 & 1) ^ 1) * 8;
        const unsigned boff10 = lds + 32768 + wn * 8192 + krow + (((4 + p4) ^ sR) * 16) + (p4 & 1) * 8;
        const unsigned boff11 = lds + 32768 + wn * 8192 + krow + (((4 + p4) ^ sR) * 16) + ((p4 & 1) ^ 1) * 8;
        f32x4 acc[4][4];
#pragma unroll
        for (int m = 0; m < 4; ++m)
#pragma unroll
            for (int n = 0; n < 4; ++n) acc[m][n] = (f32x4){0.f, 0.f, 0.f, 0.f};
        unsigned fts = 0;
        STAMP(2, 0);
        STAMP(2, 1);
#define K3_READ(F_A, F_B, so_, ks_) do { \
            F_B[0] = cat8(trread(LDSP(boff00 + (so_)) + (ks_) * 4096), trread(LDSP(boff00 + (so_)) + (ks_) * 4096 + 512)); \
            F_A[0] = *(LAS const bf16x8*)(LDSP(((ks_) ? aoff1 : aoff0) + (so_))); \
            F_B[1] = cat8(trread(LDSP(boff01 + (so_)) + (ks_) * 4096), trread(LDSP(boff01 + (so_)) + (ks_) * 4096 + 512)); \
            F_A[1] = *(LAS const bf16x8*)(LDSP(((ks_) ? aoff1 : aoff0) + (so_)) + 2048); \
            F_B[2] = cat8(trread(LDSP(boff10 + (so_)) + (ks_) * 4096), trread(LDSP(boff10 + (so_)) + (ks_) * 4096 + 512)); \
            F_A[2] = *(LAS const bf16x8*)(LDSP(((ks_) ? aoff1 : aoff0) + (so_)) + 4096); \
            F_B[3] = cat8(trread(LDSP(boff11 + (so_)) + (ks_) * 4096), trread(LDSP(boff11 + (so_)) + (ks_) * 4096 + 512)); \
            F_A[3] = *(LAS const bf16x8*)(LDSP(((ks_) ? aoff1 : aoff0) + (so_)) + 6144); } while (0)
#define K3_RB(dst, boff, so_, ks_) dst = cat8(trread(LDSP((boff) + (so_)) + (ks_) * 4096), trread(LDSP((boff) + (so_)) + (ks_) * 4096 + 512))
#define K3_RA(dst, so_, ks_, m_) dst = *(LAS const bf16x8*)(LDSP(((ks_) ? aoff1 : aoff0) + (so_)) + (m_) * 2048)
#define K3_M(CA, CB, m_, n_) acc[m_][n_] = MFMA16(CB[n_], CA[m_], acc[m_][n_])
#define K3_STEP(CA, CB, NA, NB, so_, ks_) do { \
            K3_M(CA, CB, 0, 0); K3_RA(NA[0], so_, ks_, 0); SCHED(); \
            K3_M(CA, CB, 0, 1); K3_RB(NB[0], boff00, so_, ks_); SCHED(); \
            K3_M(CA, CB, 0, 2); SCHED(); \
            K3_M(CA, CB, 0, 3); K3_RB(NB[1], boff01, so_, ks_); SCHED(); \
            K3_M(CA, CB, 1, 0); SCHED(); \
            K3_M(CA, CB, 1, 1); K3_RB(NB[2], boff10, so_, ks_); SCHED(); \
            K3_M(CA, CB, 1, 2); SCHED(); \
            K3_M(CA, CB, 1, 3); K3_RB(NB[3], boff11, so_, ks_); SCHED(); \
            K3_M(CA, CB, 2, 0); SCHED(); \
            K3_M(CA, CB, 2, 1); K3_RA(NA[1], so_, ks_, 1); SCHED(); \
            K3_M(CA, CB, 2, 2); K3_RA(NA[2], so_, ks_, 2); SCHED(); \
            K3_M(CA, CB, 2, 3); K3_RA(NA[3], so_, ks_, 3); SCHED(); \
            K3_M(CA, CB, 3, 0); K3_M(CA, CB, 3, 1); K3_M(CA, CB, 3, 2); K3_M(CA, CB, 3, 3); SCHED(); } while (0)
        bf16x8 a0[4], b0[4], a1[4], b1[4];
        K3_READ(a0, b0, 0u, 0);
        for (int t = 0; t < 18; ++t) {
            const unsigned so = (t % 3) * K3_STG, sn = ((t + 1) % 3) * K3_STG;
            if (t == 14) {
#pragma unroll
                for (int m = 0; m < 4; ++m)
#pragma unroll
                    for (int n = 0; n < 4; ++n) acc[m][n] = acc[m][n] * omg[m];
            }
            SCHED();
            K3_STEP(a0, b0, a1, b1, so, 1);
            if (t == 3) WAIT_V(0);
            WAIT_L(0); SCHED(); BAR(); SCHED();
            K3_STEP(a1, b1, a0, b0, sn, 0);
        }
#undef K3_STEP
#undef K3_M
#undef K3_RA
#undef K3_RB
#undef K3_READ
        STAMP(2, 2);
        {
            const __amdgpu_buffer_rsrc_t rs = __builtin_amdgcn_make_buffer_rsrc((void*)(ws + WS_P), (short)0, (int)(256 * 65536), 0x00020000);
            const unsigned so = (unsigned)(bid * 65536 + wid * 2048 + lane * 16);
            f32x4 fin[4];
#define K3_SEND(q) do { \
                const u32x4 w0 = {cvtpk(acc[q][0][0], acc[q][0][1]), cvtpk(acc[q][0][2], acc[q][0][3]), cvtpk(acc[q][1][0], acc[q][1][1]), cvtpk(acc[q][1][2], acc[q][1][3])}; \
                const u32x4 w1 = {cvtpk(acc[q][2][0], acc[q][2][1]), cvtpk(acc[q][2][2], acc[q][2][3]), cvtpk(acc[q][3][0], acc[q][3][1]), cvtpk(acc[q][3][2], acc[q][3][3])}; \
                __builtin_amdgcn_raw_buffer_store_b128(w0, rs, so + (q) * 16384, 0, 16); __builtin_amdgcn_raw_buffer_store_b128(w1, rs, so + (q) * 16384 + 1024, 0, 16); } while (0)
#define K3_KEEP(q) do { _Pragma("unroll") for (int n = 0; n < 4; ++n) fin[n] = acc[q][n]; } while (0)
            if (kq == 0) { K3_SEND(1); K3_SEND(2); K3_SEND(3); K3_KEEP(0); }
            else if (kq == 1) { K3_SEND(0); K3_SEND(2); K3_SEND(3); K3_KEEP(1); }
            else if (kq == 2) { K3_SEND(0); K3_SEND(1); K3_SEND(3); K3_KEEP(2); }
            else { K3_SEND(0); K3_SEND(1); K3_SEND(2); K3_KEEP(3); }
#undef K3_SEND
#undef K3_KEEP
            STAMP(2, 3);
            WAIT_V(0);
            STAMP(2, 4);
            unsigned* xcnt = (unsigned*)(ws + WS_XC);
            if (lane < 3) { const int q = (kq + 1 + lane) & 3; __hip_atomic_fetch_add(xcnt + ((idx * 8 + 2 * q + (xcd & 1)) * 8 + wid) * 32, 1u, RLX_AGENT); }
            {
                unsigned spins = 0;
                while (__builtin_amdgcn_readfirstlane(__hip_atomic_load(xcnt + (bid * 8 + wid) * 32, RLX_AGENT)) < 3u) { __builtin_amdgcn_s_sleep(1); if (++spins > (1u << 22)) break; }
            }
            asm volatile("" ::: "memory");
            STAMP(2, 5);
            u32x4 rv[3][2];
#pragma unroll
            for (int i = 0; i < 3; ++i) {
                const int q = (kq + 1 + i) & 3;
                const unsigned po = (unsigned)((idx * 8 + 2 * q + (xcd & 1)) * 65536 + kq * 16384 + wid * 2048 + lane * 16);
                rv[i][0] = __builtin_amdgcn_raw_buffer_load_b128(rs, po, 0, 16); rv[i][1] = __builtin_amdgcn_raw_buffer_load_b128(rs, po + 1024, 0, 16);
            }
#pragma unroll
            for (int i = 0; i < 3; ++i)
#pragma unroll
                for (int n = 0; n < 4; ++n) {
                    const unsigned u0 = rv[i][n >> 1][(n & 1) * 2], u1 = rv[i][n >> 1][(n & 1) * 2 + 1];
                    fin[n][0] += __uint_as_float(u0 << 16); fin[n][1] += __uint_as_float(u0 & 0xffff0000u);
                    fin[n][2] += __uint_as_float(u1 << 16); fin[n][3] += __uint_as_float(u1 & 0xffff0000u);
                }
            const int row = mb * 256 + G * 128 + wm * 64 + kq * 16 + fr;
            const bool odd = fq & 1;
#pragma unroll
            for (int pr = 0; pr < 2; ++pr) {
                const f32x4 v0 = fin[2 * pr], v1 = fin[2 * pr + 1];
                const unsigned a0 = cvtpk(v0[0], v0[1]), a1 = cvtpk(v0[2], v0[3]), b0 = cvtpk(v1[0], v1[1]), b1 = cvtpk(v1[2], v1[3]);
                u32x4 w; w.x = odd ? b0 : a0; w.y = odd ? b1 : a1; w.z = odd ? a0 : b0; w.w = odd ? a1 : b1;
                *(u32x4*)(out + (size_t)row * DM + nb * 128 + wn * 64 + pr * 32 + 8 * fq) = w;
            }
        }
        STAMP(2, 6);
        WAIT_V(0);
        STAMP(2, 7);
    }
    CODE_LABEL("gm_k23_end");
}

extern "C" void kernel_launch(void* const* d_in, const int* in_sizes, int n_in, void* d_out, int out_size, void* d_ws, size_t ws_size, hipStream_t stream) {
    const bf16_t* x = (const bf16_t*)d_in[0];
    const bf16_t* wqkv = (const bf16_t*)d_in[1];
    const bf16_t* wo = (const bf16_t*)d_in[2];
    const bf16_t* wg = (const bf16_t*)d_in[4];
    const bf16_t* wu = (const bf16_t*)d_in[5];
    const bf16_t* wd = (const bf16_t*)d_in[6];
    const bf16_t* gw = (const bf16_t*)d_in[7];
    unsigned char* ws = (unsigned char*)d_ws;
    (void)hipFuncSetAttribute((const void*)k1_gemm, hipFuncAttributeMaxDynamicSharedMemorySize, K1_LDS);
    (void)hipFuncSetAttribute((const void*)k23, hipFuncAttributeMaxDynamicSharedMemorySize, K23_LDS);
    k1_gemm<<<256, 512, K1_LDS, stream>>>(x, wqkv, wg, wu, gw, ws);
    k23<<<256, 768, K23_LDS, stream>>>(wo, wd, ws, (bf16_t*)d_out);
}
```

```cpp
#include <hip/hip_runtime.h>
#include <hip/hip_bf16.h>
#include <stdint.h>

#define LAS __attribute__((address_space(3)))
typedef unsigned short bf16_t;
typedef short bf16x8 __attribute__((ext_vector_type(8)));
typedef short s16x4 __attribute__((ext_vector_type(4)));
typedef short v4i16_t __attribute__((ext_vector_type(4)));
typedef float f32x2 __attribute__((ext_vector_type(2)));
typedef float f32x4 __attribute__((ext_vector_type(4)));
typedef float f32x16 __attribute__((ext_vector_type(16)));
typedef unsigned u32x2 __attribute__((ext_vector_type(2)));
typedef unsigned u32x4 __attribute__((ext_vector_type(4)));
typedef __bf16 bf16x2_t __attribute__((ext_vector_type(2)));

constexpr int SEQ = 2048, DM = 1024, NQKV = 3072, FF = 3584, NH = 32, DH = 32;
constexpr int KTOT = FF + DM;
constexpr size_t WS_FLAG = 0;
constexpr size_t WS_G = 131072;
constexpr size_t WS_XC = 262144;
constexpr size_t WS_Q = 1u << 20;
constexpr size_t WS_K = WS_Q + (4u << 20);
constexpr size_t WS_V = WS_K + (4u << 20);
constexpr size_t WS_A = WS_V + (4u << 20);
constexpr size_t WS_P = WS_A + (20u << 20);

#define WAIT_V(n) asm volatile("s_waitcnt vmcnt(" #n ")" ::: "memory")
#define WAIT_L(n) asm volatile("s_waitcnt lgkmcnt(" #n ")" ::: "memory")
#define BAR() asm volatile("s_barrier" ::: "memory")
#define SCHED() __builtin_amdgcn_sched_barrier(0)
#define PIN1(x) asm volatile("" : "+v"(x))
#define RLX_AGENT __ATOMIC_RELAXED, __HIP_MEMORY_SCOPE_AGENT
#define SPIN_CAP (1u << 17)
#define STAMP(kern, i) do {} while (0)

__device__ __forceinline__ unsigned cvtpk(float lo, float hi) { f32x2 v = {lo, hi}; bf16x2_t b = __builtin_convertvector(v, bf16x2_t); return __builtin_bit_cast(unsigned, b); }
__device__ __forceinline__ float bf2f(unsigned short b) { return __uint_as_float(((unsigned)b) << 16); }
__device__ __forceinline__ s16x4 trread(LAS const char* p) { return __builtin_bit_cast(s16x4, __builtin_amdgcn_ds_read_tr16_b64_v4i16((LAS v4i16_t*)p)); }
__device__ __forceinline__ bf16x8 cat8(s16x4 lo, s16x4 hi) { return (bf16x8){lo[0], lo[1], lo[2], lo[3], hi[0], hi[1], hi[2], hi[3]}; }
__device__ __forceinline__ void glds16(const char* sbase, unsigned voff, unsigned ldsaddr) {
    asm volatile("s_mov_b32 m0, %2\n\ts_nop 0\n\tglobal_load_lds_dwordx4 %0, %1" :: "v"(voff), "s"(sbase), "s"(ldsaddr) : "memory"); }
__device__ __forceinline__ void glds16_sc1(const char* sbase, unsigned voff, unsigned ldsaddr) {
    asm volatile("s_mov_b32 m0, %2\n\ts_nop 0\n\tglobal_load_lds_dwordx4 %0, %1 sc1" :: "v"(voff), "s"(sbase), "s"(ldsaddr) : "memory"); }
#define LDSP(a) ((LAS const char*)(uintptr_t)(unsigned)(a))
#define CODE_LABEL(name) asm volatile(name ":")
#define CODE_RANGE(base, len, tag, lbeg, lend) do { unsigned long long _pc; int _d; \
    asm volatile("s_getpc_b64 %0\n" tag ":\n\ts_mov_b32 %1, " lbeg "-" tag "\n\ts_mov_b32 %2, " lend "-" lbeg : "=s"(_pc), "=s"(_d), "=s"(len)); \
    base = (const char*)(_pc + (long long)_d); } while (0)
__device__ __forceinline__ float gelu_tanh(float x) {
    const float c0 = -2.0f * 0.7978845608028654f * 1.4426950408889634f, c1 = c0 * 0.044715f;
    const float z = x * __builtin_fmaf(x * x, c1, c0);
    const float e = __builtin_amdgcn_exp2f(z);
    return x * __builtin_amdgcn_rcpf(1.0f + e);
}

__device__ __forceinline__ f32x2 geglu2(f32x2 g, f32x2 u) {
    const float c0 = -2.0f * 0.7978845608028654f * 1.4426950408889634f, c1 = c0 * 0.044715f;
    const f32x2 t = __builtin_elementwise_fma(g * g, (f32x2){c1, c1}, (f32x2){c0, c0});
    const f32x2 z = g * t;
    f32x2 e; e.x = __builtin_amdgcn_exp2f(z.x); e.y = __builtin_amdgcn_exp2f(z.y);
    const f32x2 d = e + (f32x2){1.0f, 1.0f};
    f32x2 r; r.x = __builtin_amdgcn_rcpf(d.x); r.y = __builtin_amdgcn_rcpf(d.y);
    return (g * u) * r;
}

constexpr int K1_GW = 2 * 32768 + 2 * 40960;
constexpr int K1_LDS = K1_GW + 2048;
constexpr int K1_ST_AUX = 0;
constexpr float QSCALE = 0.17677669529663687f * 1.4426950408889634f;

__global__ __launch_bounds__(512, 2) void k1_gemm(const bf16_t* __restrict__ x, const bf16_t* __restrict__ wqkv, const bf16_t* __restrict__ wg,
                                                  const bf16_t* __restrict__ wu, const bf16_t* __restrict__ gate_w, unsigned char* __restrict__ ws) {
    extern __shared__ __attribute__((aligned(16))) char smem[];
    const unsigned lds = (unsigned)(uintptr_t)(LAS char*)smem;
    const int tid = threadIdx.x, wid = __builtin_amdgcn_readfirstlane(tid >> 6), lane = tid & 63;
    const int wr = wid >> 2, wc = wid & 3, fr = lane & 15, fq = lane >> 4;
    const int bid = blockIdx.x, xcd = bid & 7, idx = bid >> 3, ctl = idx & 3, rb = idx >> 2;
    const bool type1 = ctl < 3;
    const int t1 = xcd * 3 + ctl, t2 = xcd;
    const int gam0 = type1 ? (ctl < 2 ? 2 * xcd + ctl : 16 + 2 * xcd) : 17 + 2 * xcd, gam1 = 32 + 2 * xcd, gam2 = 33 + 2 * xcd;
    const char* gp0; const char* gp1; const char* gp2; const char* gp3; const char* gp4;
    if (type1) {
        gp0 = (const char*)(wg + 128 * t1); gp1 = (const char*)(wg + 128 * t1 + 64); gp2 = (const char*)(wu + 128 * t1); gp3 = (const char*)(wu + 128 * t1 + 64);
        gp4 = (const char*)(wqkv + 64 * gam0);
    } else {
        gp0 = (const char*)(wg + 3072 + 64 * t2); gp1 = (const char*)(wu + 3072 + 64 * t2);
        gp2 = (const char*)(wqkv + 64 * gam0); gp3 = (const char*)(wqkv + 64 * gam1); gp4 = (const char*)(wqkv + 64 * gam2);
    }
    const unsigned ld23 = type1 ? FF * 2 : NQKV * 2;
    const int lr = lane >> 3, ls = lane & 7;
    const unsigned voffA = (unsigned)((8 * wc + lr) * (DM * 2) + ((ls ^ ((4 * (wc & 1) + (lane >> 4)) & 7)) * 16));
    const int sB = 3 * ((lane >> 4) & 1) + 4 * (wc & 1);
    const unsigned voffG = (unsigned)((8 * wc + lr) * (FF * 2) + ((ls ^ sB) * 16));
    const unsigned voffQ = (unsigned)((8 * wc + lr) * (NQKV * 2) + ((ls ^ sB) * 16));
    const unsigned voff23 = type1 ? voffG : voffQ;
    const char* xa = (const char*)x + (size_t)rb * 256 * DM * 2;
#define K1_DMA_A(h, tt, bb) do { const char* _s = xa + (size_t)(h) * 128 * DM * 2 + (size_t)(tt) * 128; \
        _Pragma("unroll") for (int _u = 0; _u < 4; ++_u) glds16(_s + (size_t)_u * 32 * DM * 2, voffA, lds + (bb) * 32768 + ((h) * 128 + _u * 32 + wc * 8) * 128); } while (0)
#define K1_DMA_B(hb, tt, bb) do { const size_t _k = (size_t)(tt) * 64 + (hb) * 32; const unsigned _d = lds + 65536 + (bb) * 40960 + ((hb) * 32 + wc * 8) * 128; \
        glds16(gp0 + _k * (FF * 2), voffG, _d); glds16(gp1 + _k * (FF * 2), voffG, _d + 8192); \
        glds16(gp2 + _k * ld23, voff23, _d + 16384); glds16(gp3 + _k * ld23, voff23, _d + 24576); \
        glds16(gp4 + _k * (NQKV * 2), voffQ, _d + 32768); } while (0)

    STAMP(0, 0);
    const unsigned gscr = lds + 65536 + 40960 + (32 + wc * 8) * 128;
    if (wr == 1) {
        const char* xr = (const char*)x + (size_t)(rb * 256 + (xcd * 4 + ctl) * 8 + wc * 2) * (DM * 2);
#pragma unroll
        for (int i = 0; i < 4; ++i) glds16(xr + i * 1024, (unsigned)(lane * 16), gscr + i * 8192);
    }
    if (wid >= 6) glds16((const char*)gate_w + (wid - 6) * 1024, (unsigned)(lane * 16), lds + K1_GW + (wid - 6) * 1024);
    if (wr == 0) { K1_DMA_B(0, 0, 0); K1_DMA_A(1, 0, 0); K1_DMA_B(0, 1, 1); }
    else         { K1_DMA_A(0, 0, 0); K1_DMA_B(1, 0, 0); K1_DMA_A(0, 1, 1); }

    const unsigned aoff0 = lds + (wr * 128 + fr) * 128 + ((fq ^ (fr >> 1)) * 16);
    const unsigned aoff1 = lds + (wr * 128 + fr) * 128 + (((fq ^ (fr >> 1)) ^ 4) * 16);
    const int q4 = (lane >> 2) & 3, p4 = lane & 3;
    const int sR = 3 * ((q4 >> 1) & 1) + 4 * (fq & 1);
    const int krow = (8 * fq + q4) * 128;
    int gA, cA, gB, cB, gS;
    if (type1) { gA = wc >> 1; cA = 4 * (wc & 1); gB = 2 + (wc >> 1); cB = cA; gS = 4; }
    else if (wc < 2) { gA = 0; cA = 4 * wc; gB = 1; cB = cA; gS = 2; }
    else { gA = wc + 1; cA = 0; gB = wc + 1; cB = 4; gS = 2; }
    const unsigned boffA0 = lds + 65536 + gA * 8192 + krow + (((cA + p4) ^ sR) * 16) + (p4 & 1) * 8;
    const unsigned boffA1 = lds + 65536 + gA * 8192 + krow + (((cA + p4) ^ sR) * 16) + ((p4 & 1) ^ 1) * 8;
    const unsigned boffB0 = lds + 65536 + gB * 8192 + krow + (((cB + p4) ^ sR) * 16) + (p4 & 1) * 8;
    const unsigned boffB1 = lds + 65536 + gB * 8192 + krow + (((cB + p4) ^ sR) * 16) + ((p4 & 1) ^ 1) * 8;
    const unsigned boffS = lds + 65536 + gS * 8192 + krow + (((2 * wc + (p4 >> 1)) ^ sR) * 16) + (p4 & 1) * 8;

    f32x4 acc[8][5];
#pragma unroll
    for (int m = 0; m < 8; ++m)
#pragma unroll
        for (int n = 0; n < 5; ++n) acc[m][n] = (f32x4){0.f, 0.f, 0.f, 0.f};
    bf16x8 af[8], bfg[5];

    WAIT_V(9); BAR();
    STAMP(0, 1);
    if (wr == 1) {
        const u32x4 w0 = *(LAS const u32x4*)(LDSP(lds + K1_GW) + lane * 16), w1 = *(LAS const u32x4*)(LDSP(lds + K1_GW) + 1024 + lane * 16);
#pragma unroll
        for (int r = 0; r < 2; ++r) {
            const u32x4 x0 = *(LAS const u32x4*)(LDSP(gscr) + (2 * r) * 8192 + lane * 16), x1 = *(LAS const u32x4*)(LDSP(gscr) + (2 * r + 1) * 8192 + lane * 16);
            float sacc = 0.f;
#pragma unroll
            for (int i = 0; i < 4; ++i) {
                sacc = __builtin_fmaf(__uint_as_float(x0[i] << 16), __uint_as_float(w0[i] << 16), sacc);
                sacc = __builtin_fmaf(__uint_as_float(x0[i] & 0xffff0000u), __uint_as_float(w0[i] & 0xffff0000u), sacc);
                sacc = __builtin_fmaf(__uint_as_float(x1[i] << 16), __uint_as_float(w1[i] << 16), sacc);
                sacc = __builtin_fmaf(__uint_as_float(x1[i] & 0xffff0000u), __uint_as_float(w1[i] & 0xffff0000u), sacc);
            }
#pragma unroll
            for (int o = 32; o >= 1; o >>= 1) sacc += __shfl_xor(sacc, o);
            if (lane == 0) ((float*)(ws + WS_G))[rb * 256 + (xcd * 4 + ctl) * 8 + wc * 2 + r] = 1.0f / (1.0f + __expf(-sacc));
        }
        WAIT_L(0);
        BAR();
    }

#define K1_SEG(b, ks, t) do { \
        { constexpr int _bo = (b) * 40960 + (ks) * 4096; \
          bfg[0] = cat8(trread(LDSP(boffA0) + _bo), trread(LDSP(boffA0) + _bo + 512)); bfg[1] = cat8(trread(LDSP(boffA1) + _bo), trread(LDSP(boffA1) + _bo + 512)); \
          bfg[2] = cat8(trread(LDSP(boffB0) + _bo), trread(LDSP(boffB0) + _bo + 512)); bfg[3] = cat8(trread(LDSP(boffB1) + _bo), trread(LDSP(boffB1) + _bo + 512)); \
          bfg[4] = cat8(trread(LDSP(boffS) + _bo), trread(LDSP(boffS) + _bo + 512)); \
          LAS const char* _ap = LDSP((ks) ? aoff1 : aoff0) + (b) * 32768; \
          _Pragma("unroll") for (int _m = 0; _m < 8; ++_m) af[_m] = *(LAS const bf16x8*)(_ap + _m * 2048); } \
        if ((ks) == 0) { const int _tt = ((t) + 1 < 16) ? (t) + 1 : 15; if (wr == 0) K1_DMA_A(1, _tt, (b) ^ 1); else K1_DMA_B(1, _tt, (b) ^ 1); } \
        else { const int _tt = ((t) + 2 < 16) ? (t) + 2 : 15; if (wr == 0) K1_DMA_B(0, _tt, (b)); else K1_DMA_A(0, _tt, (b)); } \
        WAIT_V(9); WAIT_L(0); SCHED(); BAR(); SCHED(); \
        __builtin_amdgcn_s_setprio(1); \
        _Pragma("unroll") for (int _m = 0; _m < 8; ++_m) _Pragma("unroll") for (int _n = 0; _n < 5; ++_n) \
            acc[_m][_n] = __builtin_amdgcn_mfma_f32_16x16x32_bf16(bfg[_n], af[_m], acc[_m][_n], 0, 0, 0); \
        __builtin_amdgcn_s_setprio(0); \
        SCHED(); if (!((t) == 15 && (ks) == 1 && wr == 1)) BAR(); SCHED(); } while (0)

    for (int t = 0; t < 16; t += 2) {
        K1_SEG(0, 0, t); K1_SEG(0, 1, t); K1_SEG(1, 0, t + 1); K1_SEG(1, 1, t + 1);
    }
    STAMP(0, 2);

    const bool roleP = type1 || wc < 2;
    int lane_e = lane; asm volatile("" : "+v"(lane_e));
    {
    const int fr = lane_e & 15, fq = lane_e >> 4;
    const int row0 = rb * 256 + wr * 128 + fr;
    const bool odd = fq & 1;
    const __amdgpu_buffer_rsrc_t rs = __builtin_amdgcn_make_buffer_rsrc((void*)ws, (short)0, (int)(64u << 20), 0x00020000);
#define K1_QKV_ADDR(col0, dl, voff, step, sc) \
        const int _part = (col0) >> 10, _head = ((col0) & 1023) >> 5, _d = ((col0) & 31) + (dl); \
        const float sc = _part == 0 ? QSCALE : 1.0f; \
        unsigned voff; unsigned step; \
        if (_part == 1) { voff = (unsigned)(WS_K + ((size_t)_head * 65536 + (row0 >> 6) * 2048 + (row0 & 63) * 8) * 2) + (unsigned)(((_d >> 3) * 512 + (_d & 7)) * 2); step = 256u; } \
        else { voff = (unsigned)((_part == 0 ? WS_Q : WS_V) + ((size_t)_head * SEQ) * 64) + (unsigned)(row0 * 64 + _d * 2); step = 1024u; }
#define K1_ROWOFF(part1, m) ((part1) ? (unsigned)(((m) >> 2) * 4096 + ((m) & 3) * 256) : (unsigned)((m) * 1024))
    {
        const int col0 = 64 * gam0 + 16 * wc;
        K1_QKV_ADDR(col0, 4 * fq, voff, step, sc);
        const bool isK = step == 256u;
#pragma unroll
        for (int m = 0; m < 8; ++m) {
            const f32x4 v = acc[m][4] * sc;
            u32x2 w; w.x = cvtpk(v[0], v[1]); w.y = cvtpk(v[2], v[3]);
            __builtin_amdgcn_raw_buffer_store_b64(w, rs, voff + (isK ? K1_ROWOFF(true, m) : K1_ROWOFF(false, m)), 0, K1_ST_AUX);
        }
    }
    if (roleP) {
        const int hcol = (type1 ? 128 * t1 : 3072 + 64 * t2) + 32 * wc + 8 * fq;
        const unsigned voff = (unsigned)WS_A + (unsigned)(row0 * (KTOT * 2) + hcol * 2);
#pragma unroll
        for (int m = 0; m < 8; ++m) {
            const f32x2 h00 = geglu2((f32x2){acc[m][0][0], acc[m][0][1]}, (f32x2){acc[m][2][0], acc[m][2][1]}), h01 = geglu2((f32x2){acc[m][0][2], acc[m][0][3]}, (f32x2){acc[m][2][2], acc[m][2][3]});
            const f32x2 h10 = geglu2((f32x2){acc[m][1][0], acc[m][1][1]}, (f32x2){acc[m][3][0], acc[m][3][1]}), h11 = geglu2((f32x2){acc[m][1][2], acc[m][1][3]}, (f32x2){acc[m][3][2], acc[m][3][3]});
            const unsigned a0 = cvtpk(h00.x, h00.y), a1 = cvtpk(h01.x, h01.y), b0 = cvtpk(h10.x, h10.y), b1 = cvtpk(h11.x, h11.y);
            u32x4 w; w.x = odd ? b0 : a0; w.y = odd ? b1 : a1; w.z = odd ? a0 : b0; w.w = odd ? a1 : b1;
            __builtin_amdgcn_raw_buffer_store_b128(w, rs, voff + (unsigned)(m * 16 * KTOT * 2), 0, K1_ST_AUX);
        }
    } else {
#pragma unroll
        for (int pr = 0; pr < 2; ++pr) {
            const int col0 = 64 * (wc == 2 ? gam1 : gam2) + 32 * pr;
            K1_QKV_ADDR(col0, 8 * fq, voff, step, sc);
            const bool isK = step == 256u;
#pragma unroll
            for (int m = 0; m < 8; ++m) {
                const f32x4 v0 = acc[m][2 * pr] * sc, v1 = acc[m][2 * pr + 1] * sc;
                const unsigned a0 = cvtpk(v0[0], v0[1]), a1 = cvtpk(v0[2], v0[3]), b0 = cvtpk(v1[0], v1[1]), b1 = cvtpk(v1[2], v1[3]);
                u32x4 w; w.x = odd ? b0 : a0; w.y = odd ? b1 : a1; w.z = odd ? a0 : b0; w.w = odd ? a1 : b1;
                __builtin_amdgcn_raw_buffer_store_b128(w, rs, voff + (isK ? K1_ROWOFF(true, m) : K1_ROWOFF(false, m)), 0, K1_ST_AUX);
            }
        }
    }
#undef K1_QKV_ADDR
#undef K1_ROWOFF
    }
    if (tid == 0) ((unsigned*)(ws + WS_FLAG))[bid * 64] = 0u;
    if (tid == 64 && bid < 32) ((unsigned*)(ws + WS_FLAG))[(256 + bid) * 64] = 0u;
    if (tid >= 128 && tid < 136) ((unsigned*)(ws + WS_XC))[(bid * 8 + (tid - 128)) * 32] = 0u;
    STAMP(0, 3);
    WAIT_V(0);
    STAMP(0, 4);
#undef K1_SEG
#undef K1_DMA_A
#undef K1_DMA_B
}

constexpr int K3_STG = 49152;
constexpr int K23_SCR = 3 * K3_STG;
constexpr int K23_LDS = K23_SCR + 1024;
#define K2_RING(j) (K3_STG + (((j) + 1) % 3) * 32768)
__device__ __forceinline__ int crow(int r, int hi) { return (r & 3) + 8 * (r >> 2) + 4 * hi; }
#define MFMA32(a, b, c) __builtin_amdgcn_mfma_f32_32x32x16_bf16(a, b, c, 0, 0, 0)
#define MFMA16(a, b, c) __builtin_amdgcn_mfma_f32_16x16x32_bf16(a, b, c, 0, 0, 0)
#define MX3(a, b, c) __builtin_fmaxf(__builtin_fmaxf((a), (b)), (c))
#define PIN(x) asm volatile("" : "+v"(x))
#define EX2(v) __builtin_amdgcn_exp2f(v)
__device__ __forceinline__ void glds4_sc1(const char* sbase, unsigned voff, unsigned ldsaddr) {
    asm volatile("s_mov_b32 m0, %2\n\ts_nop 0\n\tglobal_load_lds_dword %0, %1 sc1" :: "v"(voff), "s"(sbase), "s"(ldsaddr) : "memory"); }
__device__ __forceinline__ void glds4(const char* sbase, unsigned voff, unsigned ldsaddr) {
    asm volatile("s_mov_b32 m0, %2\n\ts_nop 0\n\tglobal_load_lds_dword %0, %1" :: "v"(voff), "s"(sbase), "s"(ldsaddr) : "memory"); }

__global__ __launch_bounds__(768, 3) void k23(const bf16_t* __restrict__ wo, const bf16_t* __restrict__ wd, unsigned char* __restrict__ ws, bf16_t* __restrict__ out) {
    extern __shared__ __attribute__((aligned(16))) char smem[];
    const unsigned lds = (unsigned)(uintptr_t)(LAS char*)smem;
    const int tid = threadIdx.x, wid = __builtin_amdgcn_readfirstlane(tid >> 6), lane = tid & 63;
    const int bid = blockIdx.x, xcd = bid & 7, idx = bid >> 3;
    unsigned* flags = (unsigned*)(ws + WS_FLAG);
    const int kq = xcd >> 1, mb = (xcd & 1) * 4 + (idx >> 3), nb = idx & 7;
    const int head = xcd * 4 + (idx & 3), qb = idx >> 2;
    LAS unsigned* fl = (LAS unsigned*)(uintptr_t)(lds + K23_SCR);
    CODE_LABEL("gm_k23_beg");

    if (wid >= 8) {
        const int L = wid - 8;
        const int lr = lane >> 3, ls = lane & 7;
        const char* Kg = (const char*)(ws + WS_K) + (size_t)head * 131072;
        const char* Vg = (const char*)(ws + WS_V) + (size_t)head * 131072;
        const unsigned voff = (unsigned)(L * 4096 + lane * 16);
#define K2_DMA(j) do { const char* _k = Kg + (size_t)(j) * 16384; const char* _v = Vg + (size_t)(j) * 16384; const unsigned _d = lds + K2_RING(j) + L * 4096; \
        _Pragma("unroll") for (int _i = 0; _i < 4; ++_i) glds16(_k + _i * 1024, voff, _d + _i * 1024); \
        _Pragma("unroll") for (int _i = 0; _i < 4; ++_i) glds16(_v + _i * 1024, voff, _d + 16384 + _i * 1024); } while (0)
        const unsigned voffA0 = (unsigned)(lr * (KTOT * 2) + ((ls ^ ((lane >> 4) & 7)) * 16)), voffA1 = (unsigned)(lr * (KTOT * 2) + ((ls ^ ((4 + (lane >> 4)) & 7)) * 16));
        const unsigned voffB0 = (unsigned)(lr * (DM * 2) + ((ls ^ (3 * ((lane >> 4) & 1))) * 16)), voffB1 = (unsigned)(lr * (DM * 2) + ((ls ^ (3 * ((lane >> 4) & 1) + 4)) * 16));
        const char* Ag = (const char*)(ws + WS_A) + (size_t)(mb * 256 + L * 32) * KTOT * 2;
#define K3_DMA_A(tt, h) do { const int _t = (tt) < 18 ? (tt) : 17; const unsigned _d = lds + ((tt) % 3) * K3_STG + (h) * 16384 + L * 4096; \
        const char* _s = Ag + (_t < 14 ? (14 * kq + _t) * 128 : FF * 2 + (4 * kq + _t - 14) * 128) + (size_t)((h) * 128) * KTOT * 2; \
        if (_t < 14) { glds16(_s, voffA0, _d); glds16(_s + (size_t)8 * KTOT * 2, voffA1, _d + 1024); glds16(_s + (size_t)16 * KTOT * 2, voffA0, _d + 2048); glds16(_s + (size_t)24 * KTOT * 2, voffA1, _d + 3072); } \
        else { glds16_sc1(_s, voffA0, _d); glds16_sc1(_s + (size_t)8 * KTOT * 2, voffA1, _d + 1024); glds16_sc1(_s + (size_t)16 * KTOT * 2, voffA0, _d + 2048); glds16_sc1(_s + (size_t)24 * KTOT * 2, voffA1, _d + 3072); } } while (0)
#define K3_DMA_B(tt, g) do { const int _t = (tt) < 18 ? (tt) : 17; const unsigned _d = lds + ((tt) % 3) * K3_STG + 32768 + (g) * 8192 + L * 2048; \
        const char* _w = (_t < 14 ? (const char*)wd + (size_t)(14 * kq + _t) * 64 * DM * 2 : (const char*)wo + (size_t)(4 * kq + _t - 14) * 64 * DM * 2) + nb * 256 + (g) * 128 + (size_t)(L * 16) * DM * 2; \
        glds16(_w, voffB0, _d); glds16(_w + (size_t)8 * DM * 2, voffB1, _d + 1024); } while (0)
        K2_DMA(0); K2_DMA(1);
        {
            const char* cb; int cl;
            CODE_RANGE(cb, cl, "gm_k23_ref", "gm_k23_beg", "gm_k23_end");
            const int off = ((L & 1) * 64 + lane) * 128;
            if (L >= 2) CODE_RANGE(cb, cl, "gm_k23_ref2", "gm_k23_ref2", "gm_k23_end2");
            if (cl > 0) glds4(cb, (unsigned)(off < cl - 4 ? off : 0), lds + K23_SCR + 256);
        }
        WAIT_V(0); BAR();
#pragma unroll
        for (int j = 0; j < 8; ++j) {
            if (j == 1 || j == 2) { WAIT_V(0); BAR(); } else if (j >= 3 && j <= 5) { WAIT_V(1); BAR(); } else if (j == 6) { WAIT_V(13); BAR(); } else if (j == 7) { BAR(); }
            if (j + 2 < 8) K2_DMA(j + 2);
            if (j >= 2 && j <= 5) {
                const int ins = (j - 2) * 4 + L;
                if (ins < 9) {
                    const int row0 = (idx * 9 + ins) * 4;
                    const char* base = row0 < 896 ? (const char*)wd + (size_t)(896 * kq + row0) * 2048 : (const char*)wo + (size_t)(256 * kq + row0 - 896) * 2048;
                    glds4(base, (unsigned)(lane * 128), lds + K23_SCR + 256);
                } else {
                    const int e = ins < 14 ? ins - 9 : ins - 11;
                    const int x = e * 32 + idx;
                    const char* base = (const char*)(ws + WS_A) + (size_t)((xcd & 1) * 1024 + (x & 15) * 64) * (KTOT * 2) + (14 * kq + (x >> 4)) * 128;
                    glds4(base, (unsigned)(lane * (KTOT * 2)), lds + K23_SCR + 256);
                }
            }
            if (j == 5) { K3_DMA_A(0, 0); K3_DMA_B(0, 0); K3_DMA_B(0, 1); K3_DMA_A(0, 1); }
            if (j == 6) { K3_DMA_A(1, 0); K3_DMA_A(1, 1); }
            if (j == 7) { K3_DMA_B(1, 0); K3_DMA_B(1, 1); }
        }
        WAIT_V(0); BAR();
        const unsigned redo = __builtin_amdgcn_readfirstlane(__any(fl[lane & 7] != 0u) ? 1 : 0);
        if (redo) {
            K2_DMA(0); K2_DMA(1);
            for (int j = 0; j < 8; ++j) {
                if (j < 7) WAIT_V(8); else WAIT_V(0);
                BAR();
                if (j + 2 < 8) K2_DMA(j + 2);
            }
            BAR();
            K3_DMA_A(1, 0); K3_DMA_B(1, 0); K3_DMA_B(1, 1); K3_DMA_A(1, 1);
            WAIT_V(0); BAR();
        }
#define K3_DMA_T(tt) do { K3_DMA_A(tt, 0); K3_DMA_B(tt, 0); K3_DMA_B(tt, 1); K3_DMA_A(tt, 1); } while (0)
        K3_DMA_T(2);
        int seen = 0;
        bool repoll = false;
        for (int t = 0; t < 18; ++t) {
            if (L == 0 && t >= 4 && t <= 11) {
                if (t == 8 || t == 10 || t == 11) {
                    WAIT_V(12);
                    if (!seen) {
                        if ((unsigned)__builtin_amdgcn_readfirstlane(fl[128]) >= 8u) seen = 1;
                        else if (t == 11) {
                            unsigned spins = 0;
                            while (__builtin_amdgcn_readfirstlane(__hip_atomic_load(flags + (256 + mb * 4 + kq) * 64, RLX_AGENT)) < 8u) { __builtin_amdgcn_s_sleep(1); if (++spins > SPIN_CAP) break; }
                            seen = 1;
                        }
                    }
                } else if (t == 9 && !repoll) WAIT_V(12);
                else WAIT_V(13);
            } else if (t < 16) WAIT_V(12);
            else WAIT_V(0);
            BAR();
            if (t + 3 < 18) K3_DMA_T(t + 3);
            if (L == 0) {
                if (t == 3) { if (lane == 0) __hip_atomic_fetch_add(flags + (256 + qb * 4 + (xcd >> 1)) * 64, 1u, RLX_AGENT); }
                else if (t == 5 || (t == 8 && !seen)) { glds4_sc1((const char*)(flags + (256 + mb * 4 + kq) * 64), 0u, lds + K23_SCR + 512); repoll = t == 8; }
            }
        }
#undef K3_DMA_T
        CODE_LABEL("gm_k23_end2");
        return;
#undef K2_DMA
#undef K3_DMA_A
#undef K3_DMA_B
    }

    STAMP(1, 0);
    float omg[4];
    { const float* gv = (const float*)(ws + WS_G) + mb * 256 + (wid >> 2) * 128 + ((wid & 3) >> 1) * 64 + (lane & 15);
#pragma unroll
      for (int m = 0; m < 4; ++m) omg[m] = 1.0f - gv[16 * m]; }

    {
        const int r32 = lane & 31, hi = lane >> 5;
        const bf16_t* Qg = (const bf16_t*)(ws + WS_Q) + ((size_t)head * SEQ + qb * 256 + wid * 32) * 32;
        bf16x8 qf0 = *(const bf16x8*)(Qg + r32 * 32 + hi * 8);
        bf16x8 qf1 = *(const bf16x8*)(Qg + r32 * 32 + 16 + hi * 8);
        float gval = ((const float*)(ws + WS_G))[qb * 256 + wid * 32 + r32];
        bf16x8 ones;
        { const short o1 = (((lane >> 4) & 1) == (((lane & 15) >> 2) & 1)) ? (short)0x3F80 : (short)0;
#pragma unroll
          for (int j = 0; j < 8; ++j) ones[j] = o1; }
        const unsigned koff = lds + hi * 1024 + r32 * 16;
        const unsigned voffr = lds + 16384 + ((lane >> 4) & 1) * 32 + (lane & 3) * 8 + (4 * hi + ((lane & 15) >> 2)) * 64;
        const unsigned koff0 = koff + K2_RING(0);
        f32x16 o = {}; f32x4 lsum = {0.f, 0.f, 0.f, 0.f};
        asm volatile("" : "+v"(omg[0]), "+v"(omg[1]), "+v"(omg[2]), "+v"(omg[3]), "+v"(gval), "+v"(qf0), "+v"(qf1));
        BAR();
        STAMP(1, 1);
        {
            f32x16 SA, SB, negm;
            u32x4 WA0, WA1, WB0 = {0u, 0u, 0u, 0u}, WB1 = {0u, 0u, 0u, 0u};
            bf16x8 kf0, kf1, vf0 = {}, vf1 = {};
            kf0 = *(LAS const bf16x8*)(LDSP(koff0)); kf1 = *(LAS const bf16x8*)(LDSP(koff0) + 2048);
            { f32x16 z = {}; SA = MFMA32(kf0, qf0, z); SA = MFMA32(kf1, qf1, SA); }
            kf0 = *(LAS const bf16x8*)(LDSP(koff0) + 512); kf1 = *(LAS const bf16x8*)(LDSP(koff0) + 2048 + 512);
            {
                float a = MX3(SA[0], SA[1], SA[2]), b = MX3(SA[3], SA[4], SA[5]);
                a = MX3(a, SA[6], SA[7]); b = MX3(b, SA[8], SA[9]); a = MX3(a, SA[10], SA[11]); b = MX3(b, SA[12], SA[13]); a = MX3(a, SA[14], SA[15]);
                float rm = __builtin_fmaxf(a, b);
                auto rr = __builtin_amdgcn_permlane32_swap(__float_as_uint(rm), __float_as_uint(rm), false, false); rm = __builtin_fmaxf(__uint_as_float(rr[0]), __uint_as_float(rr[1]));
#pragma unroll
                for (int r = 0; r < 16; ++r) { SA[r] -= rm; negm[r] = -rm; }
                PIN(negm);
            }
#define K2_STEP(C, N, WP0, WP1, WC0, WC1, KADDR, VADDR) do { \
            N = MFMA32(kf0, qf0, negm); \
            C[0] = EX2(C[0]); C[1] = EX2(C[1]); C[2] = EX2(C[2]); C[3] = EX2(C[3]); PIN(C); SCHED(); \
            kf0 = *(LAS const bf16x8*)(KADDR); SCHED(); \
            o = MFMA32(vf0, __builtin_bit_cast(bf16x8, WP0), o); \
            C[4] = EX2(C[4]); C[5] = EX2(C[5]); C[6] = EX2(C[6]); C[7] = EX2(C[7]); PIN(C); SCHED(); \
            vf0 = cat8(trread(VADDR), trread((VADDR) + 512)); SCHED(); \
            lsum = MFMA16(ones, __builtin_bit_cast(bf16x8, WP0), lsum); \
            WC0[0] = cvtpk(C[0], C[1]); WC0[1] = cvtpk(C[2], C[3]); WC0[2] = cvtpk(C[4], C[5]); WC0[3] = cvtpk(C[6], C[7]); PIN(WC0); SCHED(); \
            N = MFMA32(kf1, qf1, N); \
            C[8] = EX2(C[8]); C[9] = EX2(C[9]); C[10] = EX2(C[10]); C[11] = EX2(C[11]); PIN(C); SCHED(); \
            kf1 = *(LAS const bf16x8*)((KADDR) + 2048); SCHED(); \
            o = MFMA32(vf1, __builtin_bit_cast(bf16x8, WP1), o); \
            C[12] = EX2(C[12]); C[13] = EX2(C[13]); C[14] = EX2(C[14]); C[15] = EX2(C[15]); PIN(C); SCHED(); \
            vf1 = cat8(trread((VADDR) + 1024), trread((VADDR) + 1536)); SCHED(); \
            lsum = MFMA16(ones, __builtin_bit_cast(bf16x8, WP1), lsum); \
            WC1[0] = cvtpk(C[8], C[9]); WC1[1] = cvtpk(C[10], C[11]); WC1[2] = cvtpk(C[12], C[13]); WC1[3] = cvtpk(C[14], C[15]); PIN(WC1); SCHED(); \
            } while (0)
            for (int j = 0; j < 8; ++j) {
                if (j > 0) { WAIT_L(0); BAR(); }
                LAS const char* kc = LDSP(koff + K2_RING(j));
                LAS const char* kn = LDSP(koff + K2_RING(j + 1));
                LAS const char* vc = LDSP(voffr + K2_RING(j));
                K2_STEP(SA, SB, WB0, WB1, WA0, WA1, kc + 1 * 4096, vc + 0 * 2048);
                K2_STEP(SB, SA, WA0, WA1, WB0, WB1, kc + 1 * 4096 + 512, vc + 1 * 2048);
                K2_STEP(SA, SB, WB0, WB1, WA0, WA1, kc + 2 * 4096, vc + 2 * 2048);
                K2_STEP(SB, SA, WA0, WA1, WB0, WB1, kc + 2 * 4096 + 512, vc + 3 * 2048);
                K2_STEP(SA, SB, WB0, WB1, WA0, WA1, kc + 3 * 4096, vc + 4 * 2048);
                K2_STEP(SB, SA, WA0, WA1, WB0, WB1, kc + 3 * 4096 + 512, vc + 5 * 2048);
                K2_STEP(SA, SB, WB0, WB1, WA0, WA1, kn, vc + 6 * 2048);
                K2_STEP(SB, SA, WA0, WA1, WB0, WB1, kn + 512, vc + 7 * 2048);
            }
            o = MFMA32(vf0, __builtin_bit_cast(bf16x8, WB0), o); lsum = MFMA16(ones, __builtin_bit_cast(bf16x8, WB0), lsum);
            o = MFMA32(vf1, __builtin_bit_cast(bf16x8, WB1), o); lsum = MFMA16(ones, __builtin_bit_cast(bf16x8, WB1), lsum);
#undef K2_STEP
        }
        STAMP(1, 2);
        const __amdgpu_buffer_rsrc_t rso = __builtin_amdgcn_make_buffer_rsrc((void*)(ws + WS_A), (short)0, (int)(SEQ * KTOT * 2), 0x00020000);
        const unsigned ob = (unsigned)(((qb * 256 + wid * 32 + r32) * KTOT + FF + head * 32 + 8 * hi) * 2);
#define K2_STORE_O() do { \
            const float sc = gval * __builtin_amdgcn_rcpf(lsum[0]); \
            unsigned w[8]; \
            _Pragma("unroll") for (int g = 0; g < 4; ++g) { w[2 * g] = cvtpk(o[4 * g] * sc, o[4 * g + 1] * sc); w[2 * g + 1] = cvtpk(o[4 * g + 2] * sc, o[4 * g + 3] * sc); } \
            _Pragma("unroll") for (int pr = 0; pr < 2; ++pr) { \
                auto r0 = __builtin_amdgcn_permlane32_swap(w[4 * pr], w[4 * pr + 2], false, false); \
                auto r1 = __builtin_amdgcn_permlane32_swap(w[4 * pr + 1], w[4 * pr + 3], false, false); \
                const u32x4 v = {r0[0], r1[0], r0[1], r1[1]}; \
                __builtin_amdgcn_raw_buffer_store_b128(v, rso, ob + pr * 32, 0, 16); } } while (0)
        {
            const bool bad = !(lsum[0] <= 1.0e9f);
            const unsigned badw = __any(bad) ? 1u : 0u;
            if (lane == 0) fl[wid] = badw;
            K2_STORE_O();
            STAMP(1, 3);
            WAIT_L(0); BAR();
            const unsigned redo = __builtin_amdgcn_readfirstlane(__any(fl[lane & 7] != 0u) ? 1 : 0);
            if (redo) {
                o = (f32x16){}; lsum = (f32x4){0.f, 0.f, 0.f, 0.f};
                float mhat = 0.f; f32x16 negm = {};
                bool first = true;
                for (int j = 0; j < 8; ++j) {
                    WAIT_L(0); BAR();
                    LAS const char* kb = LDSP(koff + K2_RING(j));
                    LAS const char* vb = LDSP(voffr + K2_RING(j));
#pragma unroll 1
                    for (int tt = 0; tt < 4; ++tt) {
                        LAS const char* kp = kb + tt * 4096;
                        LAS const char* vp = vb + tt * 4096;
                        const bf16x8 k00 = *(LAS const bf16x8*)(kp), k01 = *(LAS const bf16x8*)(kp + 2048), k10 = *(LAS const bf16x8*)(kp + 512), k11 = *(LAS const bf16x8*)(kp + 2048 + 512);
                        f32x16 p0 = MFMA32(k00, qf0, negm); p0 = MFMA32(k01, qf1, p0);
                        f32x16 p1 = MFMA32(k10, qf0, negm); p1 = MFMA32(k11, qf1, p1);
                        float a = MX3(p0[0], p0[1], p1[0]), b = MX3(p0[2], p0[3], p1[1]); a = MX3(a, p1[2], p1[3]);
#pragma unroll
                        for (int r = 4; r < 16; r += 4) { a = MX3(a, p0[r], p0[r + 1]); b = MX3(b, p0[r + 2], p0[r + 3]); a = MX3(a, p1[r], p1[r + 1]); b = MX3(b, p1[r + 2], p1[r + 3]); }
                        float rm = __builtin_fmaxf(a, b);
                        { auto rr = __builtin_amdgcn_permlane32_swap(__float_as_uint(rm), __float_as_uint(rm), false, false); rm = __builtin_fmaxf(__uint_as_float(rr[0]), __uint_as_float(rr[1])); }
                        {
                            const float dl = first ? rm : __builtin_fmaxf(rm, 0.f);
                            mhat += dl;
#pragma unroll
                            for (int r = 0; r < 16; ++r) { p0[r] -= dl; p1[r] -= dl; negm[r] = -mhat; }
                            if (!first) { const float f = EX2(-dl); o = o * f; lsum = lsum * f; }
                            first = false;
                        }
#pragma unroll
                        for (int r = 0; r < 16; ++r) { p0[r] = EX2(p0[r]); p1[r] = EX2(p1[r]); }
                        const u32x4 w0 = {cvtpk(p0[0], p0[1]), cvtpk(p0[2], p0[3]), cvtpk(p0[4], p0[5]), cvtpk(p0[6], p0[7])};
                        const u32x4 w1 = {cvtpk(p0[8], p0[9]), cvtpk(p0[10], p0[11]), cvtpk(p0[12], p0[13]), cvtpk(p0[14], p0[15])};
                        const u32x4 w2 = {cvtpk(p1[0], p1[1]), cvtpk(p1[2], p1[3]), cvtpk(p1[4], p1[5]), cvtpk(p1[6], p1[7])};
                        const u32x4 w3 = {cvtpk(p1[8], p1[9]), cvtpk(p1[10], p1[11]), cvtpk(p1[12], p1[13]), cvtpk(p1[14], p1[15])};
                        const bf16x8 v0 = cat8(trread(vp), trread(vp + 512)), v1 = cat8(trread(vp + 1024), trread(vp + 1536));
                        const bf16x8 v2 = cat8(trread(vp + 2048), trread(vp + 2560)), v3 = cat8(trread(vp + 3072), trread(vp + 3584));
                        const bf16x8 f0 = __builtin_bit_cast(bf16x8, w0), f1 = __builtin_bit_cast(bf16x8, w1), f2 = __builtin_bit_cast(bf16x8, w2), f3 = __builtin_bit_cast(bf16x8, w3);
                        o = MFMA32(v0, f0, o); lsum = MFMA16(ones, f0, lsum);
                        o = MFMA32(v1, f1, o); lsum = MFMA16(ones, f1, lsum);
                        o = MFMA32(v2, f2, o); lsum = MFMA16(ones, f2, lsum);
                        o = MFMA32(v3, f3, o); lsum = MFMA16(ones, f3, lsum);
                    }
                }
                WAIT_L(0); BAR();
                WAIT_V(0);
                K2_STORE_O();
                BAR();
            }
        }
        STAMP(1, 4);
#undef K2_STORE_O
    }

    {
        const int G = wid >> 2, j4 = wid & 3, wm = j4 >> 1, wn = j4 & 1, fr = lane & 15, fq = lane >> 4;
        const int arow = G * 128 + wm * 64 + fr;
        const unsigned aoff0 = lds + arow * 128 + ((fq ^ (fr >> 1)) * 16);
        const unsigned aoff1 = lds + arow * 128 + (((fq ^ (fr >> 1)) ^ 4) * 16);
        const int q4 = (lane >> 2) & 3, p4 = lane & 3;
        const int sR = 3 * ((q4 >> 1) & 1) + 4 * (fq & 1);
        const int krow = (8 * fq + q4) * 128;
        const unsigned boff00 = lds + 32768 + wn * 8192 + krow + ((p4 ^ sR) * 16) + (p4 & 1) * 8;
        const unsigned boff01 = lds + 32768 + wn * 8192 + krow + ((p4 ^ sR) * 16) + ((p4 & 1) ^ 1) * 8;
        const unsigned boff10 = lds + 32768 + wn * 8192 + krow + (((4 + p4) ^ sR) * 16) + (p4 & 1) * 8;
        const unsigned boff11 = lds + 32768 + wn * 8192 + krow + (((4 + p4) ^ sR) * 16) + ((p4 & 1) ^ 1) * 8;
        f32x4 acc[4][4];
#pragma unroll
        for (int m = 0; m < 4; ++m)
#pragma unroll
            for (int n = 0; n < 4; ++n) acc[m][n] = (f32x4){0.f, 0.f, 0.f, 0.f};
        STAMP(2, 0);
        STAMP(2, 1);
#define K3_READ(F_A, F_B, so_, ks_) do { \
            F_B[0] = cat8(trread(LDSP(boff00 + (so_)) + (ks_) * 4096), trread(LDSP(boff00 + (so_)) + (ks_) * 4096 + 512)); \
            F_A[0] = *(LAS const bf16x8*)(LDSP(((ks_) ? aoff1 : aoff0) + (so_))); \
            F_B[1] = cat8(trread(LDSP(boff01 + (so_)) + (ks_) * 4096), trread(LDSP(boff01 + (so_)) + (ks_) * 4096 + 512)); \
            F_A[1] = *(LAS const bf16x8*)(LDSP(((ks_) ? aoff1 : aoff0) + (so_)) + 2048); \
            F_B[2] = cat8(trread(LDSP(boff10 + (so_)) + (ks_) * 4096), trread(LDSP(boff10 + (so_)) + (ks_) * 4096 + 512)); \
            F_A[2] = *(LAS const bf16x8*)(LDSP(((ks_) ? aoff1 : aoff0) + (so_)) + 4096); \
            F_B[3] = cat8(trread(LDSP(boff11 + (so_)) + (ks_) * 4096), trread(LDSP(boff11 + (so_)) + (ks_) * 4096 + 512)); \
            F_A[3] = *(LAS const bf16x8*)(LDSP(((ks_) ? aoff1 : aoff0) + (so_)) + 6144); } while (0)
#define K3_RB(dst, boff, so_, ks_) dst = cat8(trread(LDSP((boff) + (so_)) + (ks_) * 4096), trread(LDSP((boff) + (so_)) + (ks_) * 4096 + 512))
#define K3_RA(dst, so_, ks_, m_) dst = *(LAS const bf16x8*)(LDSP(((ks_) ? aoff1 : aoff0) + (so_)) + (m_) * 2048)
#define K3_M(CA, CB, m_, n_) acc[m_][n_] = MFMA16(CB[n_], CA[m_], acc[m_][n_])
#define K3_STEP(CA, CB, NA, NB, so_, ks_) do { \
            K3_M(CA, CB, 0, 0); K3_RA(NA[0], so_, ks_, 0); SCHED(); \
            K3_M(CA, CB, 0, 1); K3_RB(NB[0], boff00, so_, ks_); SCHED(); \
            K3_M(CA, CB, 0, 2); SCHED(); \
            K3_M(CA, CB, 0, 3); K3_RB(NB[1], boff01, so_, ks_); SCHED(); \
            K3_M(CA, CB, 1, 0); SCHED(); \
            K3_M(CA, CB, 1, 1); K3_RB(NB[2], boff10, so_, ks_); SCHED(); \
            K3_M(CA, CB, 1, 2); SCHED(); \
            K3_M(CA, CB, 1, 3); K3_RB(NB[3], boff11, so_, ks_); SCHED(); \
            K3_M(CA, CB, 2, 0); SCHED(); \
            K3_M(CA, CB, 2, 1); K3_RA(NA[1], so_, ks_, 1); SCHED(); \
            K3_M(CA, CB, 2, 2); K3_RA(NA[2], so_, ks_, 2); SCHED(); \
            K3_M(CA, CB, 2, 3); K3_RA(NA[3], so_, ks_, 3); SCHED(); \
            K3_M(CA, CB, 3, 0); K3_M(CA, CB, 3, 1); K3_M(CA, CB, 3, 2); K3_M(CA, CB, 3, 3); SCHED(); } while (0)
        bf16x8 a0[4], b0[4], a1[4], b1[4];
        K3_READ(a0, b0, 0u, 0);
        for (int t = 0; t < 18; ++t) {
            const unsigned so = (t % 3) * K3_STG, sn = ((t + 1) % 3) * K3_STG;
            if (t == 14) {
#pragma unroll
                for (int m = 0; m < 4; ++m)
#pragma unroll
                    for (int n = 0; n < 4; ++n) acc[m][n] = acc[m][n] * omg[m];
            }
            SCHED();
            K3_STEP(a0, b0, a1, b1, so, 1);
            if (t == 3) WAIT_V(0);
            WAIT_L(0); SCHED(); BAR(); SCHED();
            K3_STEP(a1, b1, a0, b0, sn, 0);
        }
#undef K3_STEP
#undef K3_M
#undef K3_RA
#undef K3_RB
#undef K3_READ
        STAMP(2, 2);
        {
            const __amdgpu_buffer_rsrc_t rs = __builtin_amdgcn_make_buffer_rsrc((void*)(ws + WS_P), (short)0, (int)(256 * 65536), 0x00020000);
            const unsigned so = (unsigned)(bid * 65536 + wid * 2048 + lane * 16);
            f32x4 fin[4];
#define K3_SEND(q) do { \
                const u32x4 w0 = {cvtpk(acc[q][0][0], acc[q][0][1]), cvtpk(acc[q][0][2], acc[q][0][3]), cvtpk(acc[q][1][0], acc[q][1][1]), cvtpk(acc[q][1][2], acc[q][1][3])}; \
                const u32x4 w1 = {cvtpk(acc[q][2][0], acc[q][2][1]), cvtpk(acc[q][2][2], acc[q][2][3]), cvtpk(acc[q][3][0], acc[q][3][1]), cvtpk(acc[q][3][2], acc[q][3][3])}; \
                __builtin_amdgcn_raw_buffer_store_b128(w0, rs, so + (q) * 16384, 0, 16); __builtin_amdgcn_raw_buffer_store_b128(w1, rs, so + (q) * 16384 + 1024, 0, 16); } while (0)
#define K3_KEEP(q) do { _Pragma("unroll") for (int n = 0; n < 4; ++n) fin[n] = acc[q][n]; } while (0)
            if (kq == 0) { K3_SEND(1); K3_SEND(2); K3_SEND(3); K3_KEEP(0); }
            else if (kq == 1) { K3_SEND(0); K3_SEND(2); K3_SEND(3); K3_KEEP(1); }
            else if (kq == 2) { K3_SEND(0); K3_SEND(1); K3_SEND(3); K3_KEEP(2); }
            else { K3_SEND(0); K3_SEND(1); K3_SEND(2); K3_KEEP(3); }
#undef K3_SEND
#undef K3_KEEP
            STAMP(2, 3);
            WAIT_V(0);
            STAMP(2, 4);
            unsigned* xcnt = (unsigned*)(ws + WS_XC);
            if (lane < 3) { const int q = (kq + 1 + lane) & 3; __hip_atomic_fetch_add(xcnt + ((idx * 8 + 2 * q + (xcd & 1)) * 8 + wid) * 32, 1u, RLX_AGENT); }
            {
                unsigned spins = 0;
                while (__builtin_amdgcn_readfirstlane(__hip_atomic_load(xcnt + (bid * 8 + wid) * 32, RLX_AGENT)) < 3u) { __builtin_amdgcn_s_sleep(1); if (++spins > SPIN_CAP) break; }
            }
            asm volatile("" ::: "memory");
            STAMP(2, 5);
            u32x4 rv[3][2];
#pragma unroll
            for (int i = 0; i < 3; ++i) {
                const int q = (kq + 1 + i) & 3;
                const unsigned po = (unsigned)((idx * 8 + 2 * q + (xcd & 1)) * 65536 + kq * 16384 + wid * 2048 + lane * 16);
                rv[i][0] = __builtin_amdgcn_raw_buffer_load_b128(rs, po, 0, 16); rv[i][1] = __builtin_amdgcn_raw_buffer_load_b128(rs, po + 1024, 0, 16);
            }
#pragma unroll
            for (int i = 0; i < 3; ++i)
#pragma unroll
                for (int n = 0; n < 4; ++n) {
                    const unsigned u0 = rv[i][n >> 1][(n & 1) * 2], u1 = rv[i][n >> 1][(n & 1) * 2 + 1];
                    fin[n][0] += __uint_as_float(u0 << 16); fin[n][1] += __uint_as_float(u0 & 0xffff0000u);
                    fin[n][2] += __uint_as_float(u1 << 16); fin[n][3] += __uint_as_float(u1 & 0xffff0000u);
                }
            const int row = mb * 256 + G * 128 + wm * 64 + kq * 16 + fr;
            const bool odd = fq & 1;
#pragma unroll
            for (int pr = 0; pr < 2; ++pr) {
                const f32x4 v0 = fin[2 * pr], v1 = fin[2 * pr + 1];
                const unsigned a0 = cvtpk(v0[0], v0[1]), a1 = cvtpk(v0[2], v0[3]), b0 = cvtpk(v1[0], v1[1]), b1 = cvtpk(v1[2], v1[3]);
                u32x4 w; w.x = odd ? b0 : a0; w.y = odd ? b1 : a1; w.z = odd ? a0 : b0; w.w = odd ? a1 : b1;
                *(u32x4*)(out + (size_t)row * DM + nb * 128 + wn * 64 + pr * 32 + 8 * fq) = w;
            }
        }
        STAMP(2, 6);
        WAIT_V(0);
        STAMP(2, 7);
    }
    CODE_LABEL("gm_k23_end");
}

extern "C" void kernel_launch(void* const* d_in, const int* in_sizes, int n_in, void* d_out, int out_size, void* d_ws, size_t ws_size, hipStream_t stream) {
    const bf16_t* x = (const bf16_t*)d_in[0];
    const bf16_t* wqkv = (const bf16_t*)d_in[1];
    const bf16_t* wo = (const bf16_t*)d_in[2];
    const bf16_t* wg = (const bf16_t*)d_in[4];
    const bf16_t* wu = (const bf16_t*)d_in[5];
    const bf16_t* wd = (const bf16_t*)d_in[6];
    const bf16_t* gw = (const bf16_t*)d_in[7];
    unsigned char* ws = (unsigned char*)d_ws;
    (void)hipFuncSetAttribute((const void*)k1_gemm, hipFuncAttributeMaxDynamicSharedMemorySize, K1_LDS);
    (void)hipFuncSetAttribute((const void*)k23, hipFuncAttributeMaxDynamicSharedMemorySize, K23_LDS);
    k1_gemm<<<256, 512, K1_LDS, stream>>>(x, wqkv, wg, wu, gw, ws);
    k23<<<256, 768, K23_LDS, stream>>>(wo, wd, ws, (bf16_t*)d_out);
}
```

```cpp
#include <hip/hip_runtime.h>
#include <hip/hip_bf16.h>
#include <stdint.h>

#define LAS __attribute__((address_space(3)))
typedef unsigned short bf16_t;
typedef short bf16x8 __attribute__((ext_vector_type(8)));
typedef short s16x4 __attribute__((ext_vector_type(4)));
typedef short v4i16_t __attribute__((ext_vector_type(4)));
typedef float f32x2 __attribute__((ext_vector_type(2)));
typedef float f32x4 __attribute__((ext_vector_type(4)));
typedef float f32x16 __attribute__((ext_vector_type(16)));
typedef unsigned u32x2 __attribute__((ext_vector_type(2)));
typedef unsigned u32x4 __attribute__((ext_vector_type(4)));
typedef __bf16 bf16x2_t __attribute__((ext_vector_type(2)));

constexpr int SEQ = 2048, DM = 1024, NQKV = 3072, FF = 3584, NH = 32, DH = 32;
constexpr int KTOT = FF + DM;
constexpr size_t WS_FLAG = 0;
constexpr size_t WS_G = 131072;
constexpr size_t WS_XC = 262144;
constexpr size_t WS_Q = 1u << 20;
constexpr size_t WS_K = WS_Q + (4u << 20);
constexpr size_t WS_V = WS_K + (4u << 20);
constexpr size_t WS_A = WS_V + (4u << 20);
constexpr size_t WS_P = WS_A + (20u << 20);

#define WAIT_V(n) asm volatile("s_waitcnt vmcnt(" #n ")" ::: "memory")
#define WAIT_L(n) asm volatile("s_waitcnt lgkmcnt(" #n ")" ::: "memory")
#define BAR() asm volatile("s_barrier" ::: "memory")
#define SCHED() __builtin_amdgcn_sched_barrier(0)
#define PIN1(x) asm volatile("" : "+v"(x))
#define RLX_AGENT __ATOMIC_RELAXED, __HIP_MEMORY_SCOPE_AGENT
#define SPIN_CAP (1u << 17)

__device__ __forceinline__ unsigned cvtpk(float lo, float hi) { f32x2 v = {lo, hi}; bf16x2_t b = __builtin_convertvector(v, bf16x2_t); return __builtin_bit_cast(unsigned, b); }
__device__ __forceinline__ float bf2f(unsigned short b) { return __uint_as_float(((unsigned)b) << 16); }
__device__ __forceinline__ s16x4 trread(LAS const char* p) { return __builtin_bit_cast(s16x4, __builtin_amdgcn_ds_read_tr16_b64_v4i16((LAS v4i16_t*)p)); }
__device__ __forceinline__ bf16x8 cat8(s16x4 lo, s16x4 hi) { return (bf16x8){lo[0], lo[1], lo[2], lo[3], hi[0], hi[1], hi[2], hi[3]}; }
__device__ __forceinline__ void glds16(const char* sbase, unsigned voff, unsigned ldsaddr) {
    asm volatile("s_mov_b32 m0, %2\n\ts_nop 0\n\tglobal_load_lds_dwordx4 %0, %1" :: "v"(voff), "s"(sbase), "s"(ldsaddr) : "memory"); }
__device__ __forceinline__ void glds16_sc1(const char* sbase, unsigned voff, unsigned ldsaddr) {
    asm volatile("s_mov_b32 m0, %2\n\ts_nop 0\n\tglobal_load_lds_dwordx4 %0, %1 sc1" :: "v"(voff), "s"(sbase), "s"(ldsaddr) : "memory"); }
#define LDSP(a) ((LAS const char*)(uintptr_t)(unsigned)(a))
#define CODE_LABEL(name) asm volatile(name ":")
#define CODE_RANGE(base, len, tag, lbeg, lend) do { unsigned long long _pc; int _d; \
    asm volatile("s_getpc_b64 %0\n" tag ":\n\ts_mov_b32 %1, " lbeg "-" tag "\n\ts_mov_b32 %2, " lend "-" lbeg : "=s"(_pc), "=s"(_d), "=s"(len)); \
    base = (const char*)(_pc + (long long)_d); } while (0)
__device__ __forceinline__ float gelu_tanh(float x) {
    const float c0 = -2.0f * 0.7978845608028654f * 1.4426950408889634f, c1 = c0 * 0.044715f;
    const float z = x * __builtin_fmaf(x * x, c1, c0);
    const float e = __builtin_amdgcn_exp2f(z);
    return x * __builtin_amdgcn_rcpf(1.0f + e);
}

__device__ __forceinline__ f32x2 geglu2(f32x2 g, f32x2 u) {
    const float c0 = -2.0f * 0.7978845608028654f * 1.4426950408889634f, c1 = c0 * 0.044715f;
    const f32x2 t = __builtin_elementwise_fma(g * g, (f32x2){c1, c1}, (f32x2){c0, c0});
    const f32x2 z = g * t;
    f32x2 e; e.x = __builtin_amdgcn_exp2f(z.x); e.y = __builtin_amdgcn_exp2f(z.y);
    const f32x2 d = e + (f32x2){1.0f, 1.0f};
    f32x2 r; r.x = __builtin_amdgcn_rcpf(d.x); r.y = __builtin_amdgcn_rcpf(d.y);
    return (g * u) * r;
}

constexpr int K1_GW = 2 * 32768 + 2 * 40960;
constexpr int K1_LDS = K1_GW + 2048;
constexpr int K1_ST_AUX = 0;
constexpr float QSCALE = 0.17677669529663687f * 1.4426950408889634f;

__global__ __launch_bounds__(512, 2) void k1_gemm(const bf16_t* __restrict__ x, const bf16_t* __restrict__ wqkv, const bf16_t* __restrict__ wg,
                                                  const bf16_t* __restrict__ wu, const bf16_t* __restrict__ gate_w, unsigned char* __restrict__ ws) {
    extern __shared__ __attribute__((aligned(16))) char smem[];
    const unsigned lds = (unsigned)(uintptr_t)(LAS char*)smem;
    const int tid = threadIdx.x, wid = __builtin_amdgcn_readfirstlane(tid >> 6), lane = tid & 63;
    const int wr = wid >> 2, wc = wid & 3, fr = lane & 15, fq = lane >> 4;
    const int bid = blockIdx.x, xcd = bid & 7, idx = bid >> 3, ctl = idx & 3, rb = idx >> 2;
    const bool type1 = ctl < 3;
    const int t1 = xcd * 3 + ctl, t2 = xcd;
    const int gam0 = type1 ? (ctl < 2 ? 2 * xcd + ctl : 16 + 2 * xcd) : 17 + 2 * xcd, gam1 = 32 + 2 * xcd, gam2 = 33 + 2 * xcd;
    const char* gp0; const char* gp1; const char* gp2; const char* gp3; const char* gp4;
    if (type1) {
        gp0 = (const char*)(wg + 128 * t1); gp1 = (const char*)(wg + 128 * t1 + 64); gp2 = (const char*)(wu + 128 * t1); gp3 = (const char*)(wu + 128 * t1 + 64);
        gp4 = (const char*)(wqkv + 64 * gam0);
    } else {
        gp0 = (const char*)(wg + 3072 + 64 * t2); gp1 = (const char*)(wu + 3072 + 64 * t2);
        gp2 = (const char*)(wqkv + 64 * gam0); gp3 = (const char*)(wqkv + 64 * gam1); gp4 = (const char*)(wqkv + 64 * gam2);
    }
    const unsigned ld23 = type1 ? FF * 2 : NQKV * 2;
    const int lr = lane >> 3, ls = lane & 7;
    const unsigned voffA = (unsigned)((8 * wc + lr) * (DM * 2) + ((ls ^ ((4 * (wc & 1) + (lane >> 4)) & 7)) * 16));
    const int sB = 3 * ((lane >> 4) & 1) + 4 * (wc & 1);
    const unsigned voffG = (unsigned)((8 * wc + lr) * (FF * 2) + ((ls ^ sB) * 16));
    const unsigned voffQ = (unsigned)((8 * wc + lr) * (NQKV * 2) + ((ls ^ sB) * 16));
    const unsigned voff23 = type1 ? voffG : voffQ;
    const char* xa = (const char*)x + (size_t)rb * 256 * DM * 2;
#define K1_DMA_A(h, tt, bb) do { const char* _s = xa + (size_t)(h) * 128 * DM * 2 + (size_t)(tt) * 128; \
        _Pragma("unroll") for (int _u = 0; _u < 4; ++_u) glds16(_s + (size_t)_u * 32 * DM * 2, voffA, lds + (bb) * 32768 + ((h) * 128 + _u * 32 + wc * 8) * 128); } while (0)
#define K1_DMA_B(hb, tt, bb) do { const size_t _k = (size_t)(tt) * 64 + (hb) * 32; const unsigned _d = lds + 65536 + (bb) * 40960 + ((hb) * 32 + wc * 8) * 128; \
        glds16(gp0 + _k * (FF * 2), voffG, _d); glds16(gp1 + _k * (FF * 2), voffG, _d + 8192); \
        glds16(gp2 + _k * ld23, voff23, _d + 16384); glds16(gp3 + _k * ld23, voff23, _d + 24576); \
        glds16(gp4 + _k * (NQKV * 2), voffQ, _d + 32768); } while (0)

    const unsigned gscr = lds + 65536 + 40960 + (32 + wc * 8) * 128;
    if (wr == 1) {
        const char* xr = (const char*)x + (size_t)(rb * 256 + (xcd * 4 + ctl) * 8 + wc * 2) * (DM * 2);
#pragma unroll
        for (int i = 0; i < 4; ++i) glds16(xr + i * 1024, (unsigned)(lane * 16), gscr + i * 8192);
    }
    if (wid >= 6) glds16((const char*)gate_w + (wid - 6) * 1024, (unsigned)(lane * 16), lds + K1_GW + (wid - 6) * 1024);
    if (wr == 0) { K1_DMA_B(0, 0, 0); K1_DMA_A(1, 0, 0); K1_DMA_B(0, 1, 1); }
    else         { K1_DMA_A(0, 0, 0); K1_DMA_B(1, 0, 0); K1_DMA_A(0, 1, 1); }

    const unsigned aoff0 = lds + (wr * 128 + fr) * 128 + ((fq ^ (fr >> 1)) * 16);
    const unsigned aoff1 = lds + (wr * 128 + fr) * 128 + (((fq ^ (fr >> 1)) ^ 4) * 16);
    const int q4 = (lane >> 2) & 3, p4 = lane & 3;
    const int sR = 3 * ((q4 >> 1) & 1) + 4 * (fq & 1);
    const int krow = (8 * fq + q4) * 128;
    int gA, cA, gB, cB, gS;
    if (type1) { gA = wc >> 1; cA = 4 * (wc & 1); gB = 2 + (wc >> 1); cB = cA; gS = 4; }
    else if (wc < 2) { gA = 0; cA = 4 * wc; gB = 1; cB = cA; gS = 2; }
    else { gA = wc + 1; cA = 0; gB = wc + 1; cB = 4; gS = 2; }
    const unsigned boffA0 = lds + 65536 + gA * 8192 + krow + (((cA + p4) ^ sR) * 16) + (p4 & 1) * 8;
    const unsigned boffA1 = lds + 65536 + gA * 8192 + krow + (((cA + p4) ^ sR) * 16) + ((p4 & 1) ^ 1) * 8;
    const unsigned boffB0 = lds + 65536 + gB * 8192 + krow + (((cB + p4) ^ sR) * 16) + (p4 & 1) * 8;
    const unsigned boffB1 = lds + 65536 + gB * 8192 + krow + (((cB + p4) ^ sR) * 16) + ((p4 & 1) ^ 1) * 8;
    const unsigned boffS = lds + 65536 + gS * 8192 + krow + (((2 * wc + (p4 >> 1)) ^ sR) * 16) + (p4 & 1) * 8;

    f32x4 acc[8][5];
#pragma unroll
    for (int m = 0; m < 8; ++m)
#pragma unroll
        for (int n = 0; n < 5; ++n) acc[m][n] = (f32x4){0.f, 0.f, 0.f, 0.f};
    bf16x8 af[8], bfg[5];

    WAIT_V(9); BAR();
    if (wr == 1) {
        const u32x4 w0 = *(LAS const u32x4*)(LDSP(lds + K1_GW) + lane * 16), w1 = *(LAS const u32x4*)(LDSP(lds + K1_GW) + 1024 + lane * 16);
#pragma unroll
        for (int r = 0; r < 2; ++r) {
            const u32x4 x0 = *(LAS const u32x4*)(LDSP(gscr) + (2 * r) * 8192 + lane * 16), x1 = *(LAS const u32x4*)(LDSP(gscr) + (2 * r + 1) * 8192 + lane * 16);
            float sacc = 0.f;
#pragma unroll
            for (int i = 0; i < 4; ++i) {
                sacc = __builtin_fmaf(__uint_as_float(x0[i] << 16), __uint_as_float(w0[i] << 16), sacc);
                sacc = __builtin_fmaf(__uint_as_float(x0[i] & 0xffff0000u), __uint_as_float(w0[i] & 0xffff0000u), sacc);
                sacc = __builtin_fmaf(__uint_as_float(x1[i] << 16), __uint_as_float(w1[i] << 16), sacc);
                sacc = __builtin_fmaf(__uint_as_float(x1[i] & 0xffff0000u), __uint_as_float(w1[i] & 0xffff0000u), sacc);
            }
#pragma unroll
            for (int o = 32; o >= 1; o >>= 1) sacc += __shfl_xor(sacc, o);
            if (lane == 0) ((float*)(ws + WS_G))[rb * 256 + (xcd * 4 + ctl) * 8 + wc * 2 + r] = 1.0f / (1.0f + __expf(-sacc));
        }
        WAIT_L(0);
        BAR();
    }

#define K1_SEG(b, ks, t) do { \
        { constexpr int _bo = (b) * 40960 + (ks) * 4096; \
          bfg[0] = cat8(trread(LDSP(boffA0) + _bo), trread(LDSP(boffA0) + _bo + 512)); bfg[1] = cat8(trread(LDSP(boffA1) + _bo), trread(LDSP(boffA1) + _bo + 512)); \
          bfg[2] = cat8(trread(LDSP(boffB0) + _bo), trread(LDSP(boffB0) + _bo + 512)); bfg[3] = cat8(trread(LDSP(boffB1) + _bo), trread(LDSP(boffB1) + _bo + 512)); \
          bfg[4] = cat8(trread(LDSP(boffS) + _bo), trread(LDSP(boffS) + _bo + 512)); \
          LAS const char* _ap = LDSP((ks) ? aoff1 : aoff0) + (b) * 32768; \
          _Pragma("unroll") for (int _m = 0; _m < 8; ++_m) af[_m] = *(LAS const bf16x8*)(_ap + _m * 2048); } \
        if ((ks) == 0) { const int _tt = ((t) + 1 < 16) ? (t) + 1 : 15; if (wr == 0) K1_DMA_A(1, _tt, (b) ^ 1); else K1_DMA_B(1, _tt, (b) ^ 1); } \
        else { const int _tt = ((t) + 2 < 16) ? (t) + 2 : 15; if (wr == 0) K1_DMA_B(0, _tt, (b)); else K1_DMA_A(0, _tt, (b)); } \
        WAIT_V(9); WAIT_L(0); SCHED(); BAR(); SCHED(); \
        __builtin_amdgcn_s_setprio(1); \
        _Pragma("unroll") for (int _m = 0; _m < 8; ++_m) _Pragma("unroll") for (int _n = 0; _n < 5; ++_n) \
            acc[_m][_n] = __builtin_amdgcn_mfma_f32_16x16x32_bf16(bfg[_n], af[_m], acc[_m][_n], 0, 0, 0); \
        __builtin_amdgcn_s_setprio(0); \
        SCHED(); if (!((t) == 15 && (ks) == 1 && wr == 1)) BAR(); SCHED(); } while (0)

    for (int t = 0; t < 16; t += 2) {
        K1_SEG(0, 0, t); K1_SEG(0, 1, t); K1_SEG(1, 0, t + 1); K1_SEG(1, 1, t + 1);
    }

    const bool roleP = type1 || wc < 2;
    int lane_e = lane; asm volatile("" : "+v"(lane_e));
    {
    const int fr = lane_e & 15, fq = lane_e >> 4;
    const int row0 = rb * 256 + wr * 128 + fr;
    const bool odd = fq & 1;
    const __amdgpu_buffer_rsrc_t rs = __builtin_amdgcn_make_buffer_rsrc((void*)ws, (short)0, (int)(64u << 20), 0x00020000);
#define K1_QKV_ADDR(col0, dl, voff, step, sc) \
        const int _part = (col0) >> 10, _head = ((col0) & 1023) >> 5, _d = ((col0) & 31) + (dl); \
        const float sc = _part == 0 ? QSCALE : 1.0f; \
        unsigned voff; unsigned step; \
        if (_part == 1) { voff = (unsigned)(WS_K + ((size_t)_head * 65536 + (row0 >> 6) * 2048 + (row0 & 63) * 8) * 2) + (unsigned)(((_d >> 3) * 512 + (_d & 7)) * 2); step = 256u; } \
        else { voff = (unsigned)((_part == 0 ? WS_Q : WS_V) + ((size_t)_head * SEQ) * 64) + (unsigned)(row0 * 64 + _d * 2); step = 1024u; }
#define K1_ROWOFF(part1, m) ((part1) ? (unsigned)(((m) >> 2) * 4096 + ((m) & 3) * 256) : (unsigned)((m) * 1024))
    {
        const int col0 = 64 * gam0 + 16 * wc;
        K1_QKV_ADDR(col0, 4 * fq, voff, step, sc);
        const bool isK = step == 256u;
#pragma unroll
        for (int m = 0; m < 8; ++m) {
            const f32x4 v = acc[m][4] * sc;
            u32x2 w; w.x = cvtpk(v[0], v[1]); w.y = cvtpk(v[2], v[3]);
            __builtin_amdgcn_raw_buffer_store_b64(w, rs, voff + (isK ? K1_ROWOFF(true, m) : K1_ROWOFF(false, m)), 0, K1_ST_AUX);
        }
    }
    if (roleP) {
        const int hcol = (type1 ? 128 * t1 : 3072 + 64 * t2) + 32 * wc + 8 * fq;
        const unsigned voff = (unsigned)WS_A + (unsigned)(row0 * (KTOT * 2) + hcol * 2);
#pragma unroll
        for (int m = 0; m < 8; ++m) {
            const f32x2 h00 = geglu2((f32x2){acc[m][0][0], acc[m][0][1]}, (f32x2){acc[m][2][0], acc[m][2][1]}), h01 = geglu2((f32x2){acc[m][0][2], acc[m][0][3]}, (f32x2){acc[m][2][2], acc[m][2][3]});
            const f32x2 h10 = geglu2((f32x2){acc[m][1][0], acc[m][1][1]}, (f32x2){acc[m][3][0], acc[m][3][1]}), h11 = geglu2((f32x2){acc[m][1][2], acc[m][1][3]}, (f32x2){acc[m][3][2], acc[m][3][3]});
            const unsigned a0 = cvtpk(h00.x, h00.y), a1 = cvtpk(h01.x, h01.y), b0 = cvtpk(h10.x, h10.y), b1 = cvtpk(h11.x, h11.y);
            u32x4 w; w.x = odd ? b0 : a0; w.y = odd ? b1 : a1; w.z = odd ? a0 : b0; w.w = odd ? a1 : b1;
            __builtin_amdgcn_raw_buffer_store_b128(w, rs, voff + (unsigned)(m * 16 * KTOT * 2), 0, K1_ST_AUX);
        }
    } else {
#pragma unroll
        for (int pr = 0; pr < 2; ++pr) {
            const int col0 = 64 * (wc == 2 ? gam1 : gam2) + 32 * pr;
            K1_QKV_ADDR(col0, 8 * fq, voff, step, sc);
            const bool isK = step == 256u;
#pragma unroll
            for (int m = 0; m < 8; ++m) {
                const f32x4 v0 = acc[m][2 * pr] * sc, v1 = acc[m][2 * pr + 1] * sc;
                const unsigned a0 = cvtpk(v0[0], v0[1]), a1 = cvtpk(v0[2], v0[3]), b0 = cvtpk(v1[0], v1[1]), b1 = cvtpk(v1[2], v1[3]);
                u32x4 w; w.x = odd ? b0 : a0; w.y = odd ? b1 : a1; w.z = odd ? a0 : b0; w.w = odd ? a1 : b1;
                __builtin_amdgcn_raw_buffer_store_b128(w, rs, voff + (isK ? K1_ROWOFF(true, m) : K1_ROWOFF(false, m)), 0, K1_ST_AUX);
            }
        }
    }
#undef K1_QKV_ADDR
#undef K1_ROWOFF
    }
    if (tid == 0) ((unsigned*)(ws + WS_FLAG))[bid * 64] = 0u;
    if (tid == 64 && bid < 32) ((unsigned*)(ws + WS_FLAG))[(256 + bid) * 64] = 0u;
    if (tid >= 128 && tid < 136) ((unsigned*)(ws + WS_XC))[(bid * 8 + (tid - 128)) * 32] = 0u;
    WAIT_V(0);
#undef K1_SEG
#undef K1_DMA_A
#undef K1_DMA_B
}

constexpr int K3_STG = 49152;
constexpr int K23_SCR = 3 * K3_STG;
constexpr int K23_LDS = K23_SCR + 1024;
#define K2_RING(j) (K3_STG + (((j) + 1) % 3) * 32768)
__device__ __forceinline__ int crow(int r, int hi) { return (r & 3) + 8 * (r >> 2) + 4 * hi; }
#define MFMA32(a, b, c) __builtin_amdgcn_mfma_f32_32x32x16_bf16(a, b, c, 0, 0, 0)
#define MFMA16(a, b, c) __builtin_amdgcn_mfma_f32_16x16x32_bf16(a, b, c, 0, 0, 0)
#define MX3(a, b, c) __builtin_fmaxf(__builtin_fmaxf((a), (b)), (c))
#define PIN(x) asm volatile("" : "+v"(x))
#define EX2(v) __builtin_amdgcn_exp2f(v)
__device__ __forceinline__ void glds4_sc1(const char* sbase, unsigned voff, unsigned ldsaddr) {
    asm volatile("s_mov_b32 m0, %2\n\ts_nop 0\n\tglobal_load_lds_dword %0, %1 sc1" :: "v"(voff), "s"(sbase), "s"(ldsaddr) : "memory"); }
__device__ __forceinline__ void glds4(const char* sbase, unsigned voff, unsigned ldsaddr) {
    asm volatile("s_mov_b32 m0, %2\n\ts_nop 0\n\tglobal_load_lds_dword %0, %1" :: "v"(voff), "s"(sbase), "s"(ldsaddr) : "memory"); }

__global__ __launch_bounds__(768, 3) void k23(const bf16_t* __restrict__ wo, const bf16_t* __restrict__ wd, unsigned char* __restrict__ ws, bf16_t* __restrict__ out) {
    extern __shared__ __attribute__((aligned(16))) char smem[];
    const unsigned lds = (unsigned)(uintptr_t)(LAS char*)smem;
    const int tid = threadIdx.x, wid = __builtin_amdgcn_readfirstlane(tid >> 6), lane = tid & 63;
    const int bid = blockIdx.x, xcd = bid & 7, idx = bid >> 3;
    unsigned* flags = (unsigned*)(ws + WS_FLAG);
    const int kq = xcd >> 1, mb = (xcd & 1) * 4 + (idx >> 3), nb = idx & 7;
    const int head = xcd * 4 + (idx & 3), qb = idx >> 2;
    LAS unsigned* fl = (LAS unsigned*)(uintptr_t)(lds + K23_SCR);
    CODE_LABEL("gm_k23_beg");

    if (wid >= 8) {
        const int L = wid - 8;
        const int lr = lane >> 3, ls = lane & 7;
        const char* Kg = (const char*)(ws + WS_K) + (size_t)head * 131072;
        const char* Vg = (const char*)(ws + WS_V) + (size_t)head * 131072;
        const unsigned voff = (unsigned)(L * 4096 + lane * 16);
#define K2_DMA(j) do { const char* _k = Kg + (size_t)(j) * 16384; const char* _v = Vg + (size_t)(j) * 16384; const unsigned _d = lds + K2_RING(j) + L * 4096; \
        _Pragma("unroll") for (int _i = 0; _i < 4; ++_i) glds16(_k + _i * 1024, voff, _d + _i * 1024); \
        _Pragma("unroll") for (int _i = 0; _i < 4; ++_i) glds16(_v + _i * 1024, voff, _d + 16384 + _i * 1024); } while (0)
        const unsigned voffA0 = (unsigned)(lr * (KTOT * 2) + ((ls ^ ((lane >> 4) & 7)) * 16)), voffA1 = (unsigned)(lr * (KTOT * 2) + ((ls ^ ((4 + (lane >> 4)) & 7)) * 16));
        const unsigned voffB0 = (unsigned)(lr * (DM * 2) + ((ls ^ (3 * ((lane >> 4) & 1))) * 16)), voffB1 = (unsigned)(lr * (DM * 2) + ((ls ^ (3 * ((lane >> 4) & 1) + 4)) * 16));
        const char* Ag = (const char*)(ws + WS_A) + (size_t)(mb * 256 + L * 32) * KTOT * 2;
#define K3_DMA_A(tt, h) do { const int _t = (tt) < 18 ? (tt) : 17; const unsigned _d = lds + ((tt) % 3) * K3_STG + (h) * 16384 + L * 4096; \
        const char* _s = Ag + (_t < 14 ? (14 * kq + _t) * 128 : FF * 2 + (4 * kq + _t - 14) * 128) + (size_t)((h) * 128) * KTOT * 2; \
        if (_t < 14) { glds16(_s, voffA0, _d); glds16(_s + (size_t)8 * KTOT * 2, voffA1, _d + 1024); glds16(_s + (size_t)16 * KTOT * 2, voffA0, _d + 2048); glds16(_s + (size_t)24 * KTOT * 2, voffA1, _d + 3072); } \
        else { glds16_sc1(_s, voffA0, _d); glds16_sc1(_s + (size_t)8 * KTOT * 2, voffA1, _d + 1024); glds16_sc1(_s + (size_t)16 * KTOT * 2, voffA0, _d + 2048); glds16_sc1(_s + (size_t)24 * KTOT * 2, voffA1, _d + 3072); } } while (0)
#define K3_DMA_B(tt, g) do { const int _t = (tt) < 18 ? (tt) : 17; const unsigned _d = lds + ((tt) % 3) * K3_STG + 32768 + (g) * 8192 + L * 2048; \
        const char* _w = (_t < 14 ? (const char*)wd + (size_t)(14 * kq + _t) * 64 * DM * 2 : (const char*)wo + (size_t)(4 * kq + _t - 14) * 64 * DM * 2) + nb * 256 + (g) * 128 + (size_t)(L * 16) * DM * 2; \
        glds16(_w, voffB0, _d); glds16(_w + (size_t)8 * DM * 2, voffB1, _d + 1024); } while (0)
        K2_DMA(0); K2_DMA(1);
        {
            const char* cb; int cl;
            CODE_RANGE(cb, cl, "gm_k23_ref", "gm_k23_beg", "gm_k23_end");
            const int off = ((L & 1) * 64 + lane) * 128;
            if (L >= 2) CODE_RANGE(cb, cl, "gm_k23_ref2", "gm_k23_ref2", "gm_k23_end2");
            if (cl > 0) glds4(cb, (unsigned)(off < cl - 4 ? off : 0), lds + K23_SCR + 256);
        }
        WAIT_V(0); BAR();
#pragma unroll
        for (int j = 0; j < 8; ++j) {
            if (j == 1 || j == 2) { WAIT_V(0); BAR(); } else if (j >= 3 && j <= 5) { WAIT_V(1); BAR(); } else if (j == 6) { WAIT_V(13); BAR(); } else if (j == 7) { BAR(); }
            if (j + 2 < 8) K2_DMA(j + 2);
            if (j >= 2 && j <= 5) {
                const int ins = (j - 2) * 4 + L;
                if (ins < 9) {
                    const int row0 = (idx * 9 + ins) * 4;
                    const char* base = row0 < 896 ? (const char*)wd + (size_t)(896 * kq + row0) * 2048 : (const char*)wo + (size_t)(256 * kq + row0 - 896) * 2048;
                    glds4(base, (unsigned)(lane * 128), lds + K23_SCR + 256);
                } else {
                    const int e = ins < 14 ? ins - 9 : ins - 11;
                    const int x = e * 32 + idx;
                    const char* base = (const char*)(ws + WS_A) + (size_t)((xcd & 1) * 1024 + (x & 15) * 64) * (KTOT * 2) + (14 * kq + (x >> 4)) * 128;
                    glds4(base, (unsigned)(lane * (KTOT * 2)), lds + K23_SCR + 256);
                }
            }
            if (j == 5) { K3_DMA_A(0, 0); K3_DMA_B(0, 0); K3_DMA_B(0, 1); K3_DMA_A(0, 1); }
            if (j == 6) { K3_DMA_A(1, 0); K3_DMA_A(1, 1); }
            if (j == 7) { K3_DMA_B(1, 0); K3_DMA_B(1, 1); }
        }
        WAIT_V(0); BAR();
        const unsigned redo = __builtin_amdgcn_readfirstlane(__any(fl[lane & 7] != 0u) ? 1 : 0);
        if (redo) {
            K2_DMA(0); K2_DMA(1);
            for (int j = 0; j < 8; ++j) {
                if (j < 7) WAIT_V(8); else WAIT_V(0);
                BAR();
                if (j + 2 < 8) K2_DMA(j + 2);
            }
            BAR();
            K3_DMA_A(1, 0); K3_DMA_B(1, 0); K3_DMA_B(1, 1); K3_DMA_A(1, 1);
            WAIT_V(0); BAR();
        }
#define K3_DMA_T(tt) do { K3_DMA_A(tt, 0); K3_DMA_B(tt, 0); K3_DMA_B(tt, 1); K3_DMA_A(tt, 1); } while (0)
        K3_DMA_T(2);
        int seen = 0;
        bool repoll = false;
        for (int t = 0; t < 18; ++t) {
            if (L == 0 && t >= 4 && t <= 11) {
                if (t == 8 || t == 10 || t == 11) {
                    WAIT_V(12);
                    if (!seen) {
                        if ((unsigned)__builtin_amdgcn_readfirstlane(fl[128]) >= 8u) seen = 1;
                        else if (t == 11) {
                            unsigned spins = 0;
                            while (__builtin_amdgcn_readfirstlane(__hip_atomic_load(flags + (256 + mb * 4 + kq) * 64, RLX_AGENT)) < 8u) { __builtin_amdgcn_s_sleep(1); if (++spins > SPIN_CAP) break; }
                            seen = 1;
                        }
                    }
                } else if (t == 9 && !repoll) WAIT_V(12);
                else WAIT_V(13);
            } else if (t < 16) WAIT_V(12);
            else WAIT_V(0);
            BAR();
            if (t + 3 < 18) K3_DMA_T(t + 3);
            if (L == 0) {
                if (t == 3) { if (lane == 0) __hip_atomic_fetch_add(flags + (256 + qb * 4 + (xcd >> 1)) * 64, 1u, RLX_AGENT); }
                else if (t == 5 || (t == 8 && !seen)) { glds4_sc1((const char*)(flags + (256 + mb * 4 + kq) * 64), 0u, lds + K23_SCR + 512); repoll = t == 8; }
            }
        }
#undef K3_DMA_T
        CODE_LABEL("gm_k23_end2");
        return;
#undef K2_DMA
#undef K3_DMA_A
#undef K3_DMA_B
    }

    float omg[4];
    { const float* gv = (const float*)(ws + WS_G) + mb * 256 + (wid >> 2) * 128 + ((wid & 3) >> 1) * 64 + (lane & 15);
#pragma unroll
      for (int m = 0; m < 4; ++m) omg[m] = 1.0f - gv[16 * m]; }

    {
        const int r32 = lane & 31, hi = lane >> 5;
        const bf16_t* Qg = (const bf16_t*)(ws + WS_Q) + ((size_t)head * SEQ + qb * 256 + wid * 32) * 32;
        bf16x8 qf0 = *(const bf16x8*)(Qg + r32 * 32 + hi * 8);
        bf16x8 qf1 = *(const bf16x8*)(Qg + r32 * 32 + 16 + hi * 8);
        float gval = ((const float*)(ws + WS_G))[qb * 256 + wid * 32 + r32];
        bf16x8 ones;
        { const short o1 = (((lane >> 4) & 1) == (((lane & 15) >> 2) & 1)) ? (short)0x3F80 : (short)0;
#pragma unroll
          for (int j = 0; j < 8; ++j) ones[j] = o1; }
        const unsigned koff = lds + hi * 1024 + r32 * 16;
        const unsigned voffr = lds + 16384 + ((lane >> 4) & 1) * 32 + (lane & 3) * 8 + (4 * hi + ((lane & 15) >> 2)) * 64;
        const unsigned koff0 = koff + K2_RING(0);
        f32x16 o = {}; f32x4 lsum = {0.f, 0.f, 0.f, 0.f};
        asm volatile("" : "+v"(omg[0]), "+v"(omg[1]), "+v"(omg[2]), "+v"(omg[3]), "+v"(gval), "+v"(qf0), "+v"(qf1));
        BAR();
        {
            f32x16 SA, SB, negm;
            u32x4 WA0, WA1, WB0 = {0u, 0u, 0u, 0u}, WB1 = {0u, 0u, 0u, 0u};
            bf16x8 kf0, kf1, vf0 = {}, vf1 = {};
            kf0 = *(LAS const bf16x8*)(LDSP(koff0)); kf1 = *(LAS const bf16x8*)(LDSP(koff0) + 2048);
            { f32x16 z = {}; SA = MFMA32(kf0, qf0, z); SA = MFMA32(kf1, qf1, SA); }
            kf0 = *(LAS const bf16x8*)(LDSP(koff0) + 512); kf1 = *(LAS const bf16x8*)(LDSP(koff0) + 2048 + 512);
            {
                float a = MX3(SA[0], SA[1], SA[2]), b = MX3(SA[3], SA[4], SA[5]);
                a = MX3(a, SA[6], SA[7]); b = MX3(b, SA[8], SA[9]); a = MX3(a, SA[10], SA[11]); b = MX3(b, SA[12], SA[13]); a = MX3(a, SA[14], SA[15]);
                float rm = __builtin_fmaxf(a, b);
                auto rr = __builtin_amdgcn_permlane32_swap(__float_as_uint(rm), __float_as_uint(rm), false, false); rm = __builtin_fmaxf(__uint_as_float(rr[0]), __uint_as_float(rr[1]));
#pragma unroll
                for (int r = 0; r < 16; ++r) { SA[r] -= rm; negm[r] = -rm; }
                PIN(negm);
            }
#define K2_STEP(C, N, WP0, WP1, WC0, WC1, KADDR, VADDR) do { \
            N = MFMA32(kf0, qf0, negm); \
            C[0] = EX2(C[0]); C[1] = EX2(C[1]); C[2] = EX2(C[2]); C[3] = EX2(C[3]); PIN(C); SCHED(); \
            kf0 = *(LAS const bf16x8*)(KADDR); SCHED(); \
            o = MFMA32(vf0, __builtin_bit_cast(bf16x8, WP0), o); \
            C[4] = EX2(C[4]); C[5] = EX2(C[5]); C[6] = EX2(C[6]); C[7] = EX2(C[7]); PIN(C); SCHED(); \
            vf0 = cat8(trread(VADDR), trread((VADDR) + 512)); SCHED(); \
            lsum = MFMA16(ones, __builtin_bit_cast(bf16x8, WP0), lsum); \
            WC0[0] = cvtpk(C[0], C[1]); WC0[1] = cvtpk(C[2], C[3]); WC0[2] = cvtpk(C[4], C[5]); WC0[3] = cvtpk(C[6], C[7]); PIN(WC0); SCHED(); \
            N = MFMA32(kf1, qf1, N); \
            C[8] = EX2(C[8]); C[9] = EX2(C[9]); C[10] = EX2(C[10]); C[11] = EX2(C[11]); PIN(C); SCHED(); \
            kf1 = *(LAS const bf16x8*)((KADDR) + 2048); SCHED(); \
            o = MFMA32(vf1, __builtin_bit_cast(bf16x8, WP1), o); \
            C[12] = EX2(C[12]); C[13] = EX2(C[13]); C[14] = EX2(C[14]); C[15] = EX2(C[15]); PIN(C); SCHED(); \
            vf1 = cat8(trread((VADDR) + 1024), trread((VADDR) + 1536)); SCHED(); \
            lsum = MFMA16(ones, __builtin_bit_cast(bf16x8, WP1), lsum); \
            WC1[0] = cvtpk(C[8], C[9]); WC1[1] = cvtpk(C[10], C[11]); WC1[2] = cvtpk(C[12], C[13]); WC1[3] = cvtpk(C[14], C[15]); PIN(WC1); SCHED(); \
            } while (0)
            for (int j = 0; j < 8; ++j) {
                if (j > 0) { WAIT_L(0); BAR(); }
                LAS const char* kc = LDSP(koff + K2_RING(j));
                LAS const char* kn = LDSP(koff + K2_RING(j + 1));
                LAS const char* vc = LDSP(voffr + K2_RING(j));
                K2_STEP(SA, SB, WB0, WB1, WA0, WA1, kc + 1 * 4096, vc + 0 * 2048);
                K2_STEP(SB, SA, WA0, WA1, WB0, WB1, kc + 1 * 4096 + 512, vc + 1 * 2048);
                K2_STEP(SA, SB, WB0, WB1, WA0, WA1, kc + 2 * 4096, vc + 2 * 2048);
                K2_STEP(SB, SA, WA0, WA1, WB0, WB1, kc + 2 * 4096 + 512, vc + 3 * 2048);
                K2_STEP(SA, SB, WB0, WB1, WA0, WA1, kc + 3 * 4096, vc + 4 * 2048);
                K2_STEP(SB, SA, WA0, WA1, WB0, WB1, kc + 3 * 4096 + 512, vc + 5 * 2048);
                K2_STEP(SA, SB, WB0, WB1, WA0, WA1, kn, vc + 6 * 2048);
                K2_STEP(SB, SA, WA0, WA1, WB0, WB1, kn + 512, vc + 7 * 2048);
            }
            o = MFMA32(vf0, __builtin_bit_cast(bf16x8, WB0), o); lsum = MFMA16(ones, __builtin_bit_cast(bf16x8, WB0), lsum);
            o = MFMA32(vf1, __builtin_bit_cast(bf16x8, WB1), o); lsum = MFMA16(ones, __builtin_bit_cast(bf16x8, WB1), lsum);
#undef K2_STEP
        }
        const __amdgpu_buffer_rsrc_t rso = __builtin_amdgcn_make_buffer_rsrc((void*)(ws + WS_A), (short)0, (int)(SEQ * KTOT * 2), 0x00020000);
        const unsigned ob = (unsigned)(((qb * 256 + wid * 32 + r32) * KTOT + FF + head * 32 + 8 * hi) * 2);
#define K2_STORE_O() do { \
            const float sc = gval * __builtin_amdgcn_rcpf(lsum[0]); \
            unsigned w[8]; \
            _Pragma("unroll") for (int g = 0; g < 4; ++g) { w[2 * g] = cvtpk(o[4 * g] * sc, o[4 * g + 1] * sc); w[2 * g + 1] = cvtpk(o[4 * g + 2] * sc, o[4 * g + 3] * sc); } \
            _Pragma("unroll") for (int pr = 0; pr < 2; ++pr) { \
                auto r0 = __builtin_amdgcn_permlane32_swap(w[4 * pr], w[4 * pr + 2], false, false); \
                auto r1 = __builtin_amdgcn_permlane32_swap(w[4 * pr + 1], w[4 * pr + 3], false, false); \
                const u32x4 v = {r0[0], r1[0], r0[1], r1[1]}; \
                __builtin_amdgcn_raw_buffer_store_b128(v, rso, ob + pr * 32, 0, 16); } } while (0)
        {
            const bool bad = !(lsum[0] <= 1.0e9f);
            const unsigned badw = __any(bad) ? 1u : 0u;
            if (lane == 0) fl[wid] = badw;
            K2_STORE_O();
            WAIT_L(0); BAR();
            const unsigned redo = __builtin_amdgcn_readfirstlane(__any(fl[lane & 7] != 0u) ? 1 : 0);
            if (redo) {
                o = (f32x16){}; lsum = (f32x4){0.f, 0.f, 0.f, 0.f};
                float mhat = 0.f; f32x16 negm = {};
                bool first = true;
                for (int j = 0; j < 8; ++j) {
                    WAIT_L(0); BAR();
                    LAS const char* kb = LDSP(koff + K2_RING(j));
                    LAS const char* vb = LDSP(voffr + K2_RING(j));
#pragma unroll 1
                    for (int tt = 0; tt < 4; ++tt) {
                        LAS const char* kp = kb + tt * 4096;
                        LAS const char* vp = vb + tt * 4096;
                        const bf16x8 k00 = *(LAS const bf16x8*)(kp), k01 = *(LAS const bf16x8*)(kp + 2048), k10 = *(LAS const bf16x8*)(kp + 512), k11 = *(LAS const bf16x8*)(kp + 2048 + 512);
                        f32x16 p0 = MFMA32(k00, qf0, negm); p0 = MFMA32(k01, qf1, p0);
                        f32x16 p1 = MFMA32(k10, qf0, negm); p1 = MFMA32(k11, qf1, p1);
                        float a = MX3(p0[0], p0[1], p1[0]), b = MX3(p0[2], p0[3], p1[1]); a = MX3(a, p1[2], p1[3]);
#pragma unroll
                        for (int r = 4; r < 16; r += 4) { a = MX3(a, p0[r], p0[r + 1]); b = MX3(b, p0[r + 2], p0[r + 3]); a = MX3(a, p1[r], p1[r + 1]); b = MX3(b, p1[r + 2], p1[r + 3]); }
                        float rm = __builtin_fmaxf(a, b);
                        { auto rr = __builtin_amdgcn_permlane32_swap(__float_as_uint(rm), __float_as_uint(rm), false, false); rm = __builtin_fmaxf(__uint_as_float(rr[0]), __uint_as_float(rr[1])); }
                        {
                            const float dl = first ? rm : __builtin_fmaxf(rm, 0.f);
                            mhat += dl;
#pragma unroll
                            for (int r = 0; r < 16; ++r) { p0[r] -= dl; p1[r] -= dl; negm[r] = -mhat; }
                            if (!first) { const float f = EX2(-dl); o = o * f; lsum = lsum * f; }
                            first = false;
                        }
#pragma unroll
                        for (int r = 0; r < 16; ++r) { p0[r] = EX2(p0[r]); p1[r] = EX2(p1[r]); }
                        const u32x4 w0 = {cvtpk(p0[0], p0[1]), cvtpk(p0[2], p0[3]), cvtpk(p0[4], p0[5]), cvtpk(p0[6], p0[7])};
                        const u32x4 w1 = {cvtpk(p0[8], p0[9]), cvtpk(p0[10], p0[11]), cvtpk(p0[12], p0[13]), cvtpk(p0[14], p0[15])};
                        const u32x4 w2 = {cvtpk(p1[0], p1[1]), cvtpk(p1[2], p1[3]), cvtpk(p1[4], p1[5]), cvtpk(p1[6], p1[7])};
                        const u32x4 w3 = {cvtpk(p1[8], p1[9]), cvtpk(p1[10], p1[11]), cvtpk(p1[12], p1[13]), cvtpk(p1[14], p1[15])};
                        const bf16x8 v0 = cat8(trread(vp), trread(vp + 512)), v1 = cat8(trread(vp + 1024), trread(vp + 1536));
                        const bf16x8 v2 = cat8(trread(vp + 2048), trread(vp + 2560)), v3 = cat8(trread(vp + 3072), trread(vp + 3584));
                        const bf16x8 f0 = __builtin_bit_cast(bf16x8, w0), f1 = __builtin_bit_cast(bf16x8, w1), f2 = __builtin_bit_cast(bf16x8, w2), f3 = __builtin_bit_cast(bf16x8, w3);
                        o = MFMA32(v0, f0, o); lsum = MFMA16(ones, f0, lsum);
                        o = MFMA32(v1, f1, o); lsum = MFMA16(ones, f1, lsum);
                        o = MFMA32(v2, f2, o); lsum = MFMA16(ones, f2, lsum);
                        o = MFMA32(v3, f3, o); lsum = MFMA16(ones, f3, lsum);
                    }
                }
                WAIT_L(0); BAR();
                WAIT_V(0);
                K2_STORE_O();
                BAR();
            }
        }
#undef K2_STORE_O
    }

    {
        const int G = wid >> 2, j4 = wid & 3, wm = j4 >> 1, wn = j4 & 1, fr = lane & 15, fq = lane >> 4;
        const int arow = G * 128 + wm * 64 + fr;
        const unsigned aoff0 = lds + arow * 128 + ((fq ^ (fr >> 1)) * 16);
        const unsigned aoff1 = lds + arow * 128 + (((fq ^ (fr >> 1)) ^ 4) * 16);
        const int q4 = (lane >> 2) & 3, p4 = lane & 3;
        const int sR = 3 * ((q4 >> 1) & 1) + 4 * (fq & 1);
        const int krow = (8 * fq + q4) * 128;
        const unsigned boff00 = lds + 32768 + wn * 8192 + krow + ((p4 ^ sR) * 16) + (p4 & 1) * 8;
        const unsigned boff01 = lds + 32768 + wn * 8192 + krow + ((p4 ^ sR) * 16) + ((p4 & 1) ^ 1) * 8;
        const unsigned boff10 = lds + 32768 + wn * 8192 + krow + (((4 + p4) ^ sR) * 16) + (p4 & 1) * 8;
        const unsigned boff11 = lds + 32768 + wn * 8192 + krow + (((4 + p4) ^ sR) * 16) + ((p4 & 1) ^ 1) * 8;
        f32x4 acc[4][4];
#pragma unroll
        for (int m = 0; m < 4; ++m)
#pragma unroll
            for (int n = 0; n < 4; ++n) acc[m][n] = (f32x4){0.f, 0.f, 0.f, 0.f};
#define K3_READ(F_A, F_B, so_, ks_) do { \
            F_B[0] = cat8(trread(LDSP(boff00 + (so_)) + (ks_) * 4096), trread(LDSP(boff00 + (so_)) + (ks_) * 4096 + 512)); \
            F_A[0] = *(LAS const bf16x8*)(LDSP(((ks_) ? aoff1 : aoff0) + (so_))); \
            F_B[1] = cat8(trread(LDSP(boff01 + (so_)) + (ks_) * 4096), trread(LDSP(boff01 + (so_)) + (ks_) * 4096 + 512)); \
            F_A[1] = *(LAS const bf16x8*)(LDSP(((ks_) ? aoff1 : aoff0) + (so_)) + 2048); \
            F_B[2] = cat8(trread(LDSP(boff10 + (so_)) + (ks_) * 4096), trread(LDSP(boff10 + (so_)) + (ks_) * 4096 + 512)); \
            F_A[2] = *(LAS const bf16x8*)(LDSP(((ks_) ? aoff1 : aoff0) + (so_)) + 4096); \
            F_B[3] = cat8(trread(LDSP(boff11 + (so_)) + (ks_) * 4096), trread(LDSP(boff11 + (so_)) + (ks_) * 4096 + 512)); \
            F_A[3] = *(LAS const bf16x8*)(LDSP(((ks_) ? aoff1 : aoff0) + (so_)) + 6144); } while (0)
#define K3_RB(dst, boff, so_, ks_) dst = cat8(trread(LDSP((boff) + (so_)) + (ks_) * 4096), trread(LDSP((boff) + (so_)) + (ks_) * 4096 + 512))
#define K3_RA(dst, so_, ks_, m_) dst = *(LAS const bf16x8*)(LDSP(((ks_) ? aoff1 : aoff0) + (so_)) + (m_) * 2048)
#define K3_M(CA, CB, m_, n_) acc[m_][n_] = MFMA16(CB[n_], CA[m_], acc[m_][n_])
#define K3_STEP(CA, CB, NA, NB, so_, ks_) do { \
            K3_M(CA, CB, 0, 0); K3_RA(NA[0], so_, ks_, 0); SCHED(); \
            K3_M(CA, CB, 0, 1); K3_RB(NB[0], boff00, so_, ks_); SCHED(); \
            K3_M(CA, CB, 0, 2); SCHED(); \
            K3_M(CA, CB, 0, 3); K3_RB(NB[1], boff01, so_, ks_); SCHED(); \
            K3_M(CA, CB, 1, 0); SCHED(); \
            K3_M(CA, CB, 1, 1); K3_RB(NB[2], boff10, so_, ks_); SCHED(); \
            K3_M(CA, CB, 1, 2); SCHED(); \
            K3_M(CA, CB, 1, 3); K3_RB(NB[3], boff11, so_, ks_); SCHED(); \
            K3_M(CA, CB, 2, 0); SCHED(); \
            K3_M(CA, CB, 2, 1); K3_RA(NA[1], so_, ks_, 1); SCHED(); \
            K3_M(CA, CB, 2, 2); K3_RA(NA[2], so_, ks_, 2); SCHED(); \
            K3_M(CA, CB, 2, 3); K3_RA(NA[3], so_, ks_, 3); SCHED(); \
            K3_M(CA, CB, 3, 0); K3_M(CA, CB, 3, 1); K3_M(CA, CB, 3, 2); K3_M(CA, CB, 3, 3); SCHED(); } while (0)
        bf16x8 a0[4], b0[4], a1[4], b1[4];
        K3_READ(a0, b0, 0u, 0);
        for (int t = 0; t < 18; ++t) {
            const unsigned so = (t % 3) * K3_STG, sn = ((t + 1) % 3) * K3_STG;
            if (t == 14) {
#pragma unroll
                for (int m = 0; m < 4; ++m)
#pragma unroll
                    for (int n = 0; n < 4; ++n) acc[m][n] = acc[m][n] * omg[m];
            }
            SCHED();
            K3_STEP(a0, b0, a1, b1, so, 1);
            if (t == 3) WAIT_V(0);
            WAIT_L(0); SCHED(); BAR(); SCHED();
            K3_STEP(a1, b1, a0, b0, sn, 0);
        }
#undef K3_STEP
#undef K3_M
#undef K3_RA
#undef K3_RB
#undef K3_READ
        {
            const __amdgpu_buffer_rsrc_t rs = __builtin_amdgcn_make_buffer_rsrc((void*)(ws + WS_P), (short)0, (int)(256 * 65536), 0x00020000);
            const unsigned so = (unsigned)(bid * 65536 + wid * 2048 + lane * 16);
            f32x4 fin[4];
#define K3_SEND(q) do { \
                const u32x4 w0 = {cvtpk(acc[q][0][0], acc[q][0][1]), cvtpk(acc[q][0][2], acc[q][0][3]), cvtpk(acc[q][1][0], acc[q][1][1]), cvtpk(acc[q][1][2], acc[q][1][3])}; \
                const u32x4 w1 = {cvtpk(acc[q][2][0], acc[q][2][1]), cvtpk(acc[q][2][2], acc[q][2][3]), cvtpk(acc[q][3][0], acc[q][3][1]), cvtpk(acc[q][3][2], acc[q][3][3])}; \
                __builtin_amdgcn_raw_buffer_store_b128(w0, rs, so + (q) * 16384, 0, 16); __builtin_amdgcn_raw_buffer_store_b128(w1, rs, so + (q) * 16384 + 1024, 0, 16); } while (0)
#define K3_KEEP(q) do { _Pragma("unroll") for (int n = 0; n < 4; ++n) fin[n] = acc[q][n]; } while (0)
            if (kq == 0) { K3_SEND(1); K3_SEND(2); K3_SEND(3); K3_KEEP(0); }
            else if (kq == 1) { K3_SEND(0); K3_SEND(2); K3_SEND(3); K3_KEEP(1); }
            else if (kq == 2) { K3_SEND(0); K3_SEND(1); K3_SEND(3); K3_KEEP(2); }
            else { K3_SEND(0); K3_SEND(1); K3_SEND(2); K3_KEEP(3); }
#undef K3_SEND
#undef K3_KEEP
            WAIT_V(0);
            unsigned* xcnt = (unsigned*)(ws + WS_XC);
            if (lane < 3) { const int q = (kq + 1 + lane) & 3; __hip_atomic_fetch_add(xcnt + ((idx * 8 + 2 * q + (xcd & 1)) * 8 + wid) * 32, 1u, RLX_AGENT); }
            {
                unsigned spins = 0;
                while (__builtin_amdgcn_readfirstlane(__hip_atomic_load(xcnt + (bid * 8 + wid) * 32, RLX_AGENT)) < 3u) { __builtin_amdgcn_s_sleep(1); if (++spins > SPIN_CAP) break; }
            }
            asm volatile("" ::: "memory");
            u32x4 rv[3][2];
#pragma unroll
            for (int i = 0; i < 3; ++i) {
                const int q = (kq + 1 + i) & 3;
                const unsigned po = (unsigned)((idx * 8 + 2 * q + (xcd & 1)) * 65536 + kq * 16384 + wid * 2048 + lane * 16);
                rv[i][0] = __builtin_amdgcn_raw_buffer_load_b128(rs, po, 0, 16); rv[i][1] = __builtin_amdgcn_raw_buffer_load_b128(rs, po + 1024, 0, 16);
            }
#pragma unroll
            for (int i = 0; i < 3; ++i)
#pragma unroll
                for (int n = 0; n < 4; ++n) {
                    const unsigned u0 = rv[i][n >> 1][(n & 1) * 2], u1 = rv[i][n >> 1][(n & 1) * 2 + 1];
                    fin[n][0] += __uint_as_float(u0 << 16); fin[n][1] += __uint_as_float(u0 & 0xffff0000u);
                    fin[n][2] += __uint_as_float(u1 << 16); fin[n][3] += __uint_as_float(u1 & 0xffff0000u);
                }
            const int row = mb * 256 + G * 128 + wm * 64 + kq * 16 + fr;
            const bool odd = fq & 1;
#pragma unroll
            for (int pr = 0; pr < 2; ++pr) {
                const f32x4 v0 = fin[2 * pr], v1 = fin[2 * pr + 1];
                const unsigned a0 = cvtpk(v0[0], v0[1]), a1 = cvtpk(v0[2], v0[3]), b0 = cvtpk(v1[0], v1[1]), b1 = cvtpk(v1[2], v1[3]);
                u32x4 w; w.x = odd ? b0 : a0; w.y = odd ? b1 : a1; w.z = odd ? a0 : b0; w.w = odd ? a1 : b1;
                *(u32x4*)(out + (size_t)row * DM + nb * 128 + wn * 64 + pr * 32 + 8 * fq) = w;
            }
        }
        WAIT_V(0);
    }
    CODE_LABEL("gm_k23_end");
}

extern "C" void kernel_launch(void* const* d_in, const int* in_sizes, int n_in, void* d_out, int out_size, void* d_ws, size_t ws_size, hipStream_t stream) {
    const bf16_t* x = (const bf16_t*)d_in[0];
    const bf16_t* wqkv = (const bf16_t*)d_in[1];
    const bf16_t* wo = (const bf16_t*)d_in[2];
    const bf16_t* wg = (const bf16_t*)d_in[4];
    const bf16_t* wu = (const bf16_t*)d_in[5];
    const bf16_t* wd = (const bf16_t*)d_in[6];
    const bf16_t* gw = (const bf16_t*)d_in[7];
    unsigned char* ws = (unsigned char*)d_ws;
    (void)hipFuncSetAttribute((const void*)k1_gemm, hipFuncAttributeMaxDynamicSharedMemorySize, K1_LDS);
    (void)hipFuncSetAttribute((const void*)k23, hipFuncAttributeMaxDynamicSharedMemorySize, K23_LDS);
    k1_gemm<<<256, 512, K1_LDS, stream>>>(x, wqkv, wg, wu, gw, ws);
    k23<<<256, 768, K23_LDS, stream>>>(wo, wd, ws, (bf16_t*)d_out);
}
```
